```python
import torch

def _softmax(x, axis=-1):
    return torch.softmax(x, dim=axis)

def _gelu(x):
    return torch.nn.functional.gelu(x, approximate='tanh')

def _silu(x):
    return torch.nn.functional.silu(x)

def _sigmoid(x):
    return torch.sigmoid(x)

def _layernorm(x, axis=-1, eps=1e-05):
    m = x.mean(dim=axis, keepdim=True)
    v = x.var(dim=axis, keepdim=True, unbiased=False)
    return (x - m) / torch.sqrt(v + eps)

def _scatter_add(tgt, idx, src, axis=0):
    return tgt.clone().index_add_(axis, idx, src)

def _segment_sum(src, seg, n):
    out = torch.zeros((n,) + src.shape[1:], dtype=src.dtype)
    return out.index_add_(0, seg, src)

def _pad(x, pw):
    flat = tuple((v for pair in reversed(pw) for v in pair))
    return torch.nn.functional.pad(x, flat)
kernel_name = 'gated_mix__grouped_kv_mqa__mixer_gmlp__prompt'

def setup_inputs():

    _w = lambda a: (lambda t: t.to(torch.bfloat16) if t.is_floating_point() else t)(torch.as_tensor(a))

    def _A_setup():
        B, S, D, H = (1, 2048, 640, 32)
        Dh = D // H
        return {'x': _w((lambda *s: torch.randn(*s, dtype=torch.bfloat16) * 0.05)(B, S, D)), 'wq': _w((lambda *s: torch.randn(*s, dtype=torch.bfloat16) * 1.6)(D, H * Dh)), 'wk': _w((lambda *s: torch.randn(*s, dtype=torch.bfloat16) * 1.6)(D, Dh)), 'wv': _w((lambda *s: torch.randn(*s, dtype=torch.bfloat16) * 0.2)(D, Dh)), 'wo': _w((lambda *s: torch.randn(*s, dtype=torch.bfloat16) * 0.2)(H * Dh, D))}
    _A_in = _A_setup()

    def _B_setup():
        B, S, D, F = (1, 2048, 640, 1728)
        Fh = F // 2
        return {'x': _w((lambda *s: torch.randn(*s, dtype=torch.bfloat16) * 0.05)(B, S, D)), 'w_in': _w((lambda *s: torch.randn(*s, dtype=torch.bfloat16) * 0.8)(D, 2 * Fh)), 'w_sp': _w(0.01 * (lambda *s: torch.randn(*s, dtype=torch.bfloat16) * 0.8)(S, S)), 'b_sp': _w(1.0 + 0.01 * (lambda *s: torch.randn(*s, dtype=torch.bfloat16) * 0.05)(S)), 'w_out': _w((lambda *s: torch.randn(*s, dtype=torch.bfloat16) * 0.05)(Fh, D))}
    _B_in = _B_setup()
    return {'A_x': _A_in['x'], 'A_wq': _A_in['wq'], 'A_wk': _A_in['wk'], 'A_wv': _A_in['wv'], 'A_wo': _A_in['wo'], 'B_x': _B_in['x'], 'B_w_in': _B_in['w_in'], 'B_w_sp': _B_in['w_sp'], 'B_b_sp': _B_in['b_sp'], 'B_w_out': _B_in['w_out'], 'gate_w': _w((lambda *s: torch.randn(*s, dtype=torch.bfloat16) * 0.05)(640) * 16.0)}

def reference(A_x, A_wq, A_wk, A_wv, A_wo, B_x, B_w_in, B_w_sp, B_b_sp, B_w_out, gate_w):

    def _A_ref(x, wq, wk, wv, wo):
        B, S, D = x.shape
        H = 32
        Dh = D // H
        q = (lambda x, *d: x.permute(*d))((x @ wq).reshape(B, S, H, Dh), 0, 2, 1, 3)
        k = x @ wk
        v = x @ wv
        scores = torch.einsum('bhqd,bkd->bhqk', q, k) * Dh ** (-0.5)
        p = _softmax(scores, axis=-1)
        o = torch.einsum('bhqk,bkd->bhqd', p, v)
        return (lambda x, *d: x.permute(*d))(o, 0, 2, 1, 3).reshape(B, S, H * Dh) @ wo

    def _B_ref(x, w_in, w_sp, b_sp, w_out):
        B, S, D = x.shape
        F = w_in.shape[-1]
        Fh = F // 2
        z = _gelu(torch.einsum('bsd,df->bsf', x, w_in))
        z1 = z[..., :Fh].clone()
        z2 = _layernorm(z[..., Fh:], axis=-1)
        g = torch.einsum('ts,bsf->btf', w_sp, z2) + b_sp[None, :, None]
        return torch.einsum('bsf,fd->bsd', z1 * g, w_out)
    x = A_x
    g = _sigmoid((x * gate_w).sum(-1, keepdims=True))
    a = _A_ref(x, A_wq, A_wk, A_wv, A_wo)
    b = _B_ref(x, B_w_in, B_w_sp, B_b_sp, B_w_out)
    return g * a + (1 - g) * b
```

```cpp
#include <hip/hip_runtime.h>
#include <hip/hip_bf16.h>
#include <stdint.h>

typedef __attribute__((ext_vector_type(8))) __bf16 bf16x8;
typedef __attribute__((ext_vector_type(4))) __bf16 bf16x4;
typedef __attribute__((ext_vector_type(4))) short s16x4;
typedef __attribute__((ext_vector_type(8))) short s16x8;
typedef __attribute__((ext_vector_type(4))) float f32x4;
typedef __attribute__((ext_vector_type(8))) float f32x8;
typedef __attribute__((ext_vector_type(16))) float f32x16;
typedef __attribute__((ext_vector_type(2))) unsigned u32x2;
typedef __attribute__((ext_vector_type(4))) unsigned u32x4;
typedef unsigned short u16;
#define LDS __attribute__((address_space(3)))
#define DI __device__ __forceinline__

constexpr int SEQ = 2048, DM = 640, FH = 864, F2 = 1728;
constexpr int ZLD = 896;
constexpr int A3LD = 1536;
constexpr int KVT = 3072;
constexpr size_t WS_Q = 0;
constexpr size_t WS_Z1 = WS_Q + (size_t)SEQ * DM * 2;
constexpr size_t WS_Z2 = WS_Z1 + (size_t)SEQ * ZLD * 2;
constexpr size_t WS_ZN = WS_Z2 + (size_t)SEQ * ZLD * 2;
constexpr size_t WS_A3 = WS_ZN + (size_t)SEQ * ZLD * 2;
constexpr size_t WS_KV = WS_A3 + (size_t)SEQ * A3LD * 2;
constexpr size_t WS_G = WS_KV + 64 * KVT;
constexpr float QSCALE = 0.22360679774997896f * 1.4426950408889634f;

DI void glds16(const void* sbase, unsigned voff, unsigned lds_dst) {
    asm volatile("s_mov_b32 m0, %2\n\ts_nop 0\n\tglobal_load_lds_dwordx4 %0, %1" ::"v"(voff), "s"(sbase), "s"(lds_dst) : "memory");
}
#define WAITV_BAR(N) asm volatile("s_waitcnt vmcnt(" #N ") lgkmcnt(0)\n\ts_barrier" ::: "memory")
template <int N> DI void waitv_bar() { asm volatile("s_waitcnt vmcnt(%0) lgkmcnt(0)\n\ts_barrier" ::"i"(N) : "memory"); }

DI bf16x8 lds_b128(unsigned a) { return *(const LDS bf16x8*)(a); }
DI s16x4 lds_b64(unsigned a) { return *(const LDS s16x4*)(a); }
DI s16x4 lds_tr(unsigned a) { return __builtin_amdgcn_ds_read_tr16_b64_v4i16((LDS s16x4*)(a)); }
DI bf16x8 cat8(s16x4 lo, s16x4 hi) { return __builtin_bit_cast(bf16x8, __builtin_shufflevector(lo, hi, 0, 1, 2, 3, 4, 5, 6, 7)); }
DI f32x4 mfma16(bf16x8 a, bf16x8 b, f32x4 c) { return __builtin_amdgcn_mfma_f32_16x16x32_bf16(a, b, c, 0, 0, 0); }
DI f32x16 mfma32(bf16x8 a, bf16x8 b, f32x16 c) { return __builtin_amdgcn_mfma_f32_32x32x16_bf16(a, b, c, 0, 0, 0); }
DI f32x16 mfma32k8(s16x4 a, s16x4 b, f32x16 c) { return __builtin_amdgcn_mfma_f32_32x32x8bf16_1k(a, b, c, 0, 0, 0); }
DI u32x2 pack4(f32x4 v) { return __builtin_bit_cast(u32x2, __builtin_convertvector(v, bf16x4)); }
DI float bf2f(u16 v) { return __uint_as_float((unsigned)v << 16); }
DI float bflo(unsigned v) { return __uint_as_float(v << 16); }
DI float bfhi(unsigned v) { return __uint_as_float(v & 0xffff0000u); }
DI float gelu_tanh(float x) {
    const float c0 = -2.0f * 0.7978845608028654f * 1.4426950408889634f, c1 = c0 * 0.044715f;
    const float e = __builtin_amdgcn_exp2f(x * __builtin_fmaf(x * x, c1, c0));
    return x * __builtin_amdgcn_rcpf(1.0f + e);
}

constexpr int K1_STAGE = 36864, K1_LDS = 4 * K1_STAGE;

__global__ __launch_bounds__(256, 1) void k1_proj(const u16* __restrict__ x, const u16* __restrict__ wq, const u16* __restrict__ wk,
                                                   const u16* __restrict__ wv, const u16* __restrict__ win, const u16* __restrict__ gw,
                                                   char* __restrict__ ws) {
    extern __shared__ __attribute__((aligned(16))) char smem[];
    const unsigned lds0 = (unsigned)(uintptr_t)smem;
    const int tid = threadIdx.x, lane = tid & 63, w = __builtin_amdgcn_readfirstlane(tid >> 6);
    const int bid = blockIdx.x, xcd = bid & 7, idx = bid >> 3;
    const int mt = (xcd & 1) * 8 + (idx & 7), nt = (xcd >> 1) * 4 + (idx >> 3);
    const int l15 = lane & 15, g = lane >> 4, q = l15 >> 2, p = lane & 3;
    const char* xb = (const char*)(x + (size_t)mt * 128 * DM);
    const unsigned xoff = (unsigned)((8 * w + (lane >> 3)) * DM * 2 + (((lane & 7) ^ ((4 * w + (lane >> 4)) & 7)) << 4));
    const unsigned xr = (unsigned)(l15 >> 1);

    if (nt < 15) {
        const int wm = w & 1, wn = w >> 1;
        const int ldw = nt < 4 ? DM : F2;
        const char* wb = (const char*)(nt < 4 ? wq + nt * 160 : win + (nt - 4) * 160);
        const int maxc = nt == 14 ? 15 : 19;
        unsigned woff[5];
#pragma unroll
        for (int j = 0; j < 5; ++j) {
            const int c = (w + 4 * j) * 64 + lane, row = c / 20, pos = c - row * 20;
            const int sc = min(pos ^ (((row >> 3) & 1) << 1), maxc);
            woff[j] = (unsigned)(row * ldw * 2 + sc * 16);
        }
        auto issue = [&](int t) {
            const unsigned b = lds0 + (t & 3) * K1_STAGE;
#pragma unroll
            for (int j = 0; j < 4; ++j) glds16(xb + t * 128 + j * 32 * DM * 2, xoff, b + (w + 4 * j) * 1024);
#pragma unroll
            for (int j = 0; j < 5; ++j) glds16(wb + (size_t)t * 64 * ldw * 2, woff[j], b + 16384 + (w + 4 * j) * 1024);
        };
        f32x4 acc[4][5];
#pragma unroll
        for (int a = 0; a < 4; ++a)
#pragma unroll
            for (int b = 0; b < 5; ++b) acc[a][b] = f32x4{0.f, 0.f, 0.f, 0.f};
        issue(0); issue(1); issue(2);
        const unsigned xa = (unsigned)((64 * wm + l15) * 128);
        const unsigned wa = (unsigned)(16384 + (8 * g + q) * 320 + p * 8);
#pragma unroll
        for (int t = 0; t < 10; ++t) {
            if (t < 8) waitv_bar<18>(); else if (t == 8) waitv_bar<9>(); else waitv_bar<0>();
            if (t + 3 < 10) issue(t + 3);
            const unsigned b = lds0 + (t & 3) * K1_STAGE;
#pragma unroll
            for (int ks = 0; ks < 2; ++ks) {
                bf16x8 xf[4], wf[5];
#pragma unroll
                for (int mi = 0; mi < 4; ++mi) xf[mi] = lds_b128(b + xa + mi * 2048 + ((((unsigned)(4 * ks + g)) ^ xr) << 4));
#pragma unroll
                for (int ni = 0; ni < 5; ++ni) {
                    const unsigned a = b + wa + ks * 32 * 320 + (unsigned)(((5 * wn + ni) ^ (g & 1)) * 32);
                    wf[ni] = cat8(lds_tr(a), lds_tr(a + 4 * 320));
                }
#pragma unroll
                for (int mi = 0; mi < 4; ++mi)
#pragma unroll
                    for (int ni = 0; ni < 5; ++ni) acc[mi][ni] = mfma16(wf[ni], xf[mi], acc[mi][ni]);
            }
        }
        const int tok0 = mt * 128 + 64 * wm + l15;
        if (nt < 4) {
            u16* qo = (u16*)(ws + WS_Q);
#pragma unroll
            for (int mi = 0; mi < 4; ++mi)
#pragma unroll
                for (int ni = 0; ni < 5; ++ni)
                    *(u32x2*)(qo + (size_t)(tok0 + 16 * mi) * DM + nt * 160 + wn * 80 + ni * 16 + 4 * g) = pack4(acc[mi][ni] * QSCALE);
        } else {
#pragma unroll
            for (int ni = 0; ni < 5; ++ni) {
                const int c0 = (nt - 4) * 160 + wn * 80 + ni * 16;
                if (c0 < F2) {
                    u16* zo = (u16*)(ws + (c0 < FH ? WS_Z1 : WS_Z2)) + (c0 < FH ? c0 : c0 - FH) + 4 * g;
#pragma unroll
                    for (int mi = 0; mi < 4; ++mi) {
                        f32x4 v = acc[mi][ni];
#pragma unroll
                        for (int r = 0; r < 4; ++r) v[r] = gelu_tanh(v[r]);
                        *(u32x2*)(zo + (size_t)(tok0 + 16 * mi) * ZLD) = pack4(v);
                    }
                }
            }
        }
    } else {
        const unsigned img = lds0 + 65536;
        auto issue = [&](int t) {
            const unsigned b = lds0 + (t & 3) * 16384;
#pragma unroll
            for (int j = 0; j < 4; ++j) glds16(xb + t * 128 + j * 32 * DM * 2, xoff, b + (w + 4 * j) * 1024);
        };
        {
            u32x2 tk[13], tv[13];
#pragma unroll
            for (int j = 0; j < 13; ++j) {
                const int c = min(tid + 256 * j, 3199);
                tk[j] = *(const u32x2*)(wk + c * 4);
                tv[j] = *(const u32x2*)(wv + c * 4);
            }
            u16 tg[3];
#pragma unroll
            for (int j = 0; j < 3; ++j) tg[j] = gw[min(tid + 256 * j, 639)];
#pragma unroll
            for (int j = 0; j < 13; ++j) {
                const int c = min(tid + 256 * j, 3199), row = c / 5, pc = c - row * 5;
                const int xs = ((row >> 1) & 1) | (((row >> 3) & 1) << 1);
                const int blk = pc >> 2, po = (pc & 3) * 8;
                *(LDS u32x2*)(img + row * 128 + ((blk ^ xs) * 32) + po) = tk[j];
                *(LDS u32x2*)(img + row * 128 + (((2 + blk) ^ xs) * 32) + po) = tv[j];
            }
#pragma unroll
            for (int j = 0; j < 3; ++j) {
                const int row = min(tid + 256 * j, 639);
                const int xs = ((row >> 1) & 1) | (((row >> 3) & 1) << 1);
                *(LDS u16*)(img + row * 128 + ((1 ^ xs) * 32) + 8) = tg[j];
            }
        }
        asm volatile("s_waitcnt vmcnt(0)" ::: "memory");
        issue(0); issue(1); issue(2);
        f32x4 acc[2][4];
#pragma unroll
        for (int a = 0; a < 2; ++a)
#pragma unroll
            for (int b = 0; b < 4; ++b) acc[a][b] = f32x4{0.f, 0.f, 0.f, 0.f};
        const unsigned xa = (unsigned)((32 * w + l15) * 128);
        const unsigned xs = (unsigned)(((q >> 1) & 1) | ((g & 1) << 1));
        const unsigned wa = img + (unsigned)((8 * g + q) * 128 + p * 8);
#pragma unroll
        for (int t = 0; t < 10; ++t) {
            if (t < 8) waitv_bar<8>(); else if (t == 8) waitv_bar<4>(); else waitv_bar<0>();
            if (t + 3 < 10) issue(t + 3);
            const unsigned b = lds0 + (t & 3) * 16384;
#pragma unroll
            for (int ks = 0; ks < 2; ++ks) {
                bf16x8 xf[2], wf[4];
#pragma unroll
                for (int mi = 0; mi < 2; ++mi) xf[mi] = lds_b128(b + xa + mi * 2048 + ((((unsigned)(4 * ks + g)) ^ xr) << 4));
#pragma unroll
                for (int ni = 0; ni < 4; ++ni) {
                    const unsigned a = wa + (t * 64 + ks * 32) * 128 + (((unsigned)ni ^ xs) * 32);
                    wf[ni] = cat8(lds_tr(a), lds_tr(a + 4 * 128));
                }
#pragma unroll
                for (int mi = 0; mi < 2; ++mi) {
                    acc[mi][0] = mfma16(wf[0], xf[mi], acc[mi][0]);
                    acc[mi][1] = mfma16(wf[1], xf[mi], acc[mi][1]);
                    acc[mi][2] = mfma16(xf[mi], wf[2], acc[mi][2]);
                    acc[mi][3] = mfma16(xf[mi], wf[3], acc[mi][3]);
                }
            }
        }
        char* kv = ws + WS_KV + (size_t)(mt * 4 + w) * KVT;
        float* go = (float*)(ws + WS_G) + mt * 128 + 32 * w;
#pragma unroll
        for (int mi = 0; mi < 2; ++mi) {
            const int key = 16 * mi + l15;
            *(u32x2*)(kv + (key + 32 * (g >> 1)) * 16 + (g & 1) * 8) = pack4(acc[mi][0]);
            if (g == 0) {
                *(u32x2*)(kv + 1024 + key * 8) = pack4(acc[mi][1]);
                *(u32x2*)(kv + 1024 + 256 + key * 8) = u32x2{0x00003f80u, 0u};
            }
            if (g == 1) go[key] = __builtin_amdgcn_rcpf(1.0f + __builtin_amdgcn_exp2f(-1.4426950408889634f * acc[mi][1][0]));
            char* vb = kv + 1536 + mi * 768 + (g & 1) * 384 + (g >> 1) * 8;
            *(u32x2*)(vb + l15 * 16) = pack4(acc[mi][2]);
            if (l15 < 8) {
                u32x2 val = pack4(acc[mi][3]);
                if (l15 == 4) val = u32x2{0x3f803f80u, 0x3f803f80u};
                if (l15 > 4) val = u32x2{0u, 0u};
                *(u32x2*)(vb + (16 + l15) * 16) = val;
            }
        }
    }
}

DI float wave_sum(float v) {
#pragma unroll
    for (int o = 32; o > 0; o >>= 1) v += __shfl_xor(v, o);
    return v;
}
DI void ln_row(const char* ws_z2, char* ws_zn, int row, int lane) {
    const u32x4* src = (const u32x4*)(ws_z2 + (size_t)row * ZLD * 2);
    const bool act = lane < 54;
    u32x4 a = u32x4{0u, 0u, 0u, 0u}, b = a;
    if (act) { a = src[2 * lane]; b = src[2 * lane + 1]; }
    float v[16];
#pragma unroll
    for (int i = 0; i < 4; ++i) { v[2 * i] = bflo(a[i]); v[2 * i + 1] = bfhi(a[i]); v[8 + 2 * i] = bflo(b[i]); v[9 + 2 * i] = bfhi(b[i]); }
    float s = 0.f;
#pragma unroll
    for (int i = 0; i < 16; ++i) s += v[i];
    const float mean = wave_sum(s) * (1.0f / FH);
    float ss = 0.f;
#pragma unroll
    for (int i = 0; i < 16; ++i) { v[i] = act ? v[i] - mean : 0.f; ss += v[i] * v[i]; }
    const float rstd = __builtin_amdgcn_rsqf(wave_sum(ss) * (1.0f / FH) + 1e-5f);
    f32x8 lo, hi;
#pragma unroll
    for (int i = 0; i < 8; ++i) { lo[i] = v[i] * rstd; hi[i] = v[8 + i] * rstd; }
    if (lane < 56) {
        bf16x8* dst = (bf16x8*)(ws_zn + (size_t)row * ZLD * 2);
        dst[2 * lane] = __builtin_convertvector(lo, bf16x8);
        dst[2 * lane + 1] = __builtin_convertvector(hi, bf16x8);
    }
}
__global__ __launch_bounds__(512) void k_ln(char* __restrict__ ws) {
    ln_row(ws + WS_Z2, ws + WS_ZN, blockIdx.x * 8 + (threadIdx.x >> 6), threadIdx.x & 63);
}

constexpr int AT_STAGE = 8 * KVT, AT_LDS = 3 * AT_STAGE;

DI float half_max(float m) {
    auto rr = __builtin_amdgcn_permlane32_swap(__float_as_uint(m), __float_as_uint(m), false, false);
    return __builtin_fmaxf(__uint_as_float(rr[0]), __uint_as_float(rr[1]));
}
DI float max16(const f32x16& s) {
    float a = __builtin_fmaxf(s[0], s[1]);
#pragma unroll
    for (int i = 2; i < 16; ++i) a = __builtin_fmaxf(a, s[i]);
    return a;
}
DI void exp_pack(const f32x16& s, bf16x8& p0, bf16x8& p1) {
    f32x8 lo, hi;
#pragma unroll
    for (int i = 0; i < 8; ++i) { lo[i] = __builtin_amdgcn_exp2f(s[i]); hi[i] = __builtin_amdgcn_exp2f(s[8 + i]); }
    p0 = __builtin_convertvector(lo, bf16x8);
    p1 = __builtin_convertvector(hi, bf16x8);
}
DI f32x16 attn_exact(const char* kv, bf16x8 q0, s16x4 q1, int lane) {
    const int r32 = lane & 31, h = lane >> 5;
    if (h) q1 = s16x4{0, 0, 0, 0};
    f32x16 o = {};
    float m = -INFINITY;
    const unsigned vo = 1536 + h * 384 + min(r32, 23) * 16;
    for (int t = 0; t < 64; ++t) {
        const char* b = kv + t * KVT;
        const bf16x8 k0 = *(const bf16x8*)(b + lane * 16);
        const s16x4 k1 = *(const s16x4*)(b + 1024 + lane * 8);
        const bf16x8 v0 = *(const bf16x8*)(b + vo), v1 = *(const bf16x8*)(b + vo + 768);
        f32x16 s = mfma32k8(k1, q1, mfma32(k0, q0, f32x16{}));
        const float mn = __builtin_fmaxf(m, half_max(max16(s)));
        const float al = __builtin_amdgcn_exp2f(m - mn);
        m = mn;
#pragma unroll
        for (int i = 0; i < 16; ++i) { s[i] -= mn; o[i] *= al; }
        bf16x8 p0, p1;
        exp_pack(s, p0, p1);
        o = mfma32(v1, p1, mfma32(v0, p0, o));
    }
    return o;
}

DI void attn_body(char* __restrict__ ws, unsigned lds0, int panel, int hg) {
    const int tid = threadIdx.x, lane = tid & 63, w = __builtin_amdgcn_readfirstlane(tid >> 6);
    const int r32 = lane & 31, h = lane >> 5, kh = w >> 2, head = hg * 4 + (w & 3);
    const char* kv = ws + WS_KV;
    const char* src = kv + (size_t)((w >> 2) * 32 + (w & 3)) * KVT;
    auto issue = [&](int t, unsigned buf) {
#pragma unroll
        for (int j = 0; j < 3; ++j) glds16(src + (size_t)t * 4 * KVT + j * 1024, (unsigned)lane * 16, buf + w * KVT + j * 1024);
    };
    issue(0, lds0);
    const u16* qp = (const u16*)(ws + WS_Q) + (size_t)(panel * 64 + r32) * DM + head * 20;
    const bf16x8 qA0 = *(const bf16x8*)(qp + 8 * h), qB0 = *(const bf16x8*)(qp + 32 * DM + 8 * h);
    s16x4 qA1 = *(const s16x4*)(qp + 16), qB1 = *(const s16x4*)(qp + 32 * DM + 16);
    const float gA = ((const float*)(ws + WS_G))[panel * 64 + 32 * kh + r32];
    asm volatile("s_waitcnt vmcnt(0)" ::: "memory");
    issue(1, lds0 + AT_STAGE);
    if (h) { qA1 = s16x4{0, 0, 0, 0}; qB1 = s16x4{0, 0, 0, 0}; }
    waitv_bar<3>();
    const unsigned ko = lane * 16, k1o = 1024 + lane * 8, vo = 1536 + h * 384 + min(r32, 23) * 16;
    {
        const bf16x8 k0 = lds_b128(lds0 + ko);
        const s16x4 k1 = lds_b64(lds0 + k1o);
        const float mA = half_max(max16(mfma32k8(k1, qA1, mfma32(k0, qA0, f32x16{}))));
        const float mB = half_max(max16(mfma32k8(k1, qB1, mfma32(k0, qB0, f32x16{}))));
        if (h) {
            qA1[0] = (short)(__float_as_uint(-mA) >> 16);
            qB1[0] = (short)(__float_as_uint(-mB) >> 16);
        }
    }
    f32x16 oA = {}, oB = {};
    unsigned cur = lds0, nxt = lds0 + 2 * AT_STAGE;
    for (int t = 0; t < 8; ++t) {
        if (t > 0) { if (t < 7) waitv_bar<3>(); else waitv_bar<0>(); }
        if (t + 2 < 8) issue(t + 2, nxt);
        const unsigned b = cur + kh * 4 * KVT;
#pragma unroll
        for (int kt = 0; kt < 4; ++kt) {
            const bf16x8 k0 = lds_b128(b + kt * KVT + ko);
            const s16x4 k1 = lds_b64(b + kt * KVT + k1o);
            const bf16x8 v0 = lds_b128(b + kt * KVT + vo), v1 = lds_b128(b + kt * KVT + vo + 768);
            const f32x16 sA = mfma32k8(k1, qA1, mfma32(k0, qA0, f32x16{}));
            const f32x16 sB = mfma32k8(k1, qB1, mfma32(k0, qB0, f32x16{}));
            bf16x8 p0, p1;
            exp_pack(sA, p0, p1);
            oA = mfma32(v1, p1, mfma32(v0, p0, oA));
            exp_pack(sB, p0, p1);
            oB = mfma32(v1, p1, mfma32(v0, p0, oB));
        }
        nxt = cur;
        cur = (cur == lds0 + 2 * AT_STAGE) ? lds0 : cur + AT_STAGE;
    }
    asm volatile("s_waitcnt lgkmcnt(0)\n\ts_barrier" ::: "memory");
    {
        const f32x16 snd = kh ? oA : oB;
        const unsigned a = lds0 + w * 3072 + lane * 16;
#pragma unroll
        for (int i = 0; i < 3; ++i) *(LDS f32x4*)(a + i * 1024) = f32x4{snd[4 * i], snd[4 * i + 1], snd[4 * i + 2], snd[4 * i + 3]};
    }
    asm volatile("s_waitcnt lgkmcnt(0)\n\ts_barrier" ::: "memory");
    f32x16 o = kh ? oB : oA;
    {
        const unsigned a = lds0 + (w ^ 4) * 3072 + lane * 16;
#pragma unroll
        for (int i = 0; i < 3; ++i) {
            const f32x4 r = *(const LDS f32x4*)(a + i * 1024);
#pragma unroll
            for (int j = 0; j < 4; ++j) o[4 * i + j] += r[j];
        }
    }
    {
        float mx = 0.f;
#pragma unroll
        for (int i = 0; i < 12; ++i) mx = __builtin_fmaxf(mx, __builtin_fabsf(o[i]));
        bool bad = false;
#pragma unroll
        for (int i = 0; i < 12; ++i) bad |= !(__builtin_fabsf(o[i]) < 1e30f);
        if (__any(bad)) o = attn_exact(kv, kh ? qB0 : qA0, kh ? qB1 : qA1, lane);
    }
    const float lsum = __shfl(o[8], r32 + 32);
    const float sc = gA * __builtin_amdgcn_rcpf(lsum);
    u16* op = (u16*)(ws + WS_A3) + (size_t)(panel * 64 + 32 * kh + r32) * A3LD + head * 20 + 4 * h;
    *(u32x2*)(op) = pack4(f32x4{o[0], o[1], o[2], o[3]} * sc);
    *(u32x2*)(op + 8) = pack4(f32x4{o[4], o[5], o[6], o[7]} * sc);
    if (h == 0) *(u32x2*)(op + 16) = pack4(f32x4{o[8], o[9], o[10], o[11]} * sc);
}
__global__ __launch_bounds__(512, 4) void k_attn(char* __restrict__ ws) {
    extern __shared__ __attribute__((aligned(16))) char smem[];
    const int bid = blockIdx.x;
    attn_body(ws, (unsigned)(uintptr_t)smem, (bid & 7) * 4 + (bid >> 6), (bid >> 3) & 7);
}

constexpr int SP_STAGE = 24576, SP_LDS = 3 * SP_STAGE;
DI void sp_body(const u16* __restrict__ wsp, const u16* __restrict__ bsp, char* __restrict__ ws, unsigned lds0, int panel, int nt) {
    const int tid = threadIdx.x, lane = tid & 63, w = __builtin_amdgcn_readfirstlane(tid >> 6);
    const int r32 = lane & 31, h = lane >> 5, q = (lane & 15) >> 2, p = lane & 3;
    const char* xb = (const char*)(wsp + (size_t)(panel * 64 + 8 * w) * SEQ);
    const unsigned xoff = (unsigned)((lane >> 3) * SEQ * 2 + (((lane & 7) ^ ((4 * w + (lane >> 4)) & 7)) << 4));
    const char* wb = ws + WS_ZN + (size_t)(8 * w) * ZLD * 2 + nt * 256;
    const unsigned woff = (unsigned)((lane >> 4) * ZLD * 2 + (((lane & 15) ^ ((lane >> 4) << 2)) << 4));
    auto issue = [&](int t, unsigned buf) {
        glds16(xb + t * 128, xoff, buf + w * 1024);
        glds16(wb + (size_t)t * 64 * ZLD * 2, woff, buf + 8192 + w * 2048);
        glds16(wb + (size_t)(t * 64 + 4) * ZLD * 2, woff, buf + 8192 + w * 2048 + 1024);
    };
    issue(0, lds0);
    issue(1, lds0 + SP_STAGE);
    const int tm = w & 1, tn = w >> 1;
    const unsigned xa = (unsigned)((32 * tm + r32) * 128), xr = (unsigned)((r32 >> 1) & 7);
    const unsigned wa = (unsigned)(8192 + (8 * h + q) * 256 + (((4 * tn + 2 * ((lane >> 4) & 1) + (p >> 1)) ^ (q << 2)) << 4) + (p & 1) * 8);
    f32x16 acc = {};
    unsigned cur = lds0, nxt = lds0 + 2 * SP_STAGE;
    for (int t = 0; t < 32; ++t) {
        if (t < 31) waitv_bar<3>(); else waitv_bar<0>();
        if (t + 2 < 32) issue(t + 2, nxt);
#pragma unroll
        for (int ks = 0; ks < 4; ++ks) {
            const bf16x8 xf = lds_b128(cur + xa + ((((unsigned)(2 * ks + h)) ^ xr) << 4));
            const unsigned a = cur + wa + ks * 16 * 256;
            acc = mfma32(cat8(lds_tr(a), lds_tr(a + 4 * 256)), xf, acc);
        }
        nxt = cur;
        cur = (cur == lds0 + 2 * SP_STAGE) ? lds0 : cur + SP_STAGE;
    }
    const int tok = panel * 64 + 32 * tm + r32;
    const float bb = bf2f(bsp[tok]), og = 1.0f - ((const float*)(ws + WS_G))[tok];
    const int f0 = nt * 128 + 32 * tn;
    if (f0 < FH) {
        const u16* z1 = (const u16*)(ws + WS_Z1) + (size_t)tok * ZLD + f0 + 4 * h;
        u16* uo = (u16*)(ws + WS_A3) + (size_t)tok * A3LD + DM + f0 + 4 * h;
#pragma unroll
        for (int gi = 0; gi < 4; ++gi) {
            const u32x2 z = *(const u32x2*)(z1 + 8 * gi);
            const f32x4 v = {(acc[4 * gi] + bb) * bflo(z[0]) * og, (acc[4 * gi + 1] + bb) * bfhi(z[0]) * og,
                             (acc[4 * gi + 2] + bb) * bflo(z[1]) * og, (acc[4 * gi + 3] + bb) * bfhi(z[1]) * og};
            *(u32x2*)(uo + 8 * gi) = pack4(v);
        }
    }
}
__global__ __launch_bounds__(512, 4) void k_sp(const u16* __restrict__ wsp, const u16* __restrict__ bsp, char* __restrict__ ws) {
    extern __shared__ __attribute__((aligned(16))) char smem[];
    const int bid = blockIdx.x, idx = bid >> 3;
    sp_body(wsp, bsp, ws, (unsigned)(uintptr_t)smem, (bid & 7) * 4 + idx / 7, idx % 7);
}

constexpr int OU_STAGE = 20480, OU_LDS = 4 * OU_STAGE;
__global__ __launch_bounds__(512, 2) void k_out(const u16* __restrict__ wo, const u16* __restrict__ wout, const char* __restrict__ ws,
                                                 u16* __restrict__ out) {
    extern __shared__ __attribute__((aligned(16))) char smem[];
    const unsigned lds0 = (unsigned)(uintptr_t)smem;
    const int bid = blockIdx.x, idx = bid >> 3, panel = (bid & 7) * 4 + idx / 7, nt = idx % 7;
    const int tid = threadIdx.x, lane = tid & 63, w = __builtin_amdgcn_readfirstlane(tid >> 6);
    const int r32 = lane & 31, h = lane >> 5, q = (lane & 15) >> 2, p = lane & 3;
    const char* xb = ws + WS_A3 + (size_t)(panel * 64 + 8 * w) * A3LD * 2;
    const unsigned xoff = (unsigned)((lane >> 3) * A3LD * 2 + (((lane & 7) ^ ((4 * w + (lane >> 4)) & 7)) << 4));
    const int maxc = nt == 6 ? 7 : 11;
    unsigned woff[2], woffl[2];
#pragma unroll
    for (int j = 0; j < 2; ++j) {
        const int c = (w + 8 * j) * 64 + lane, row = c / 12, pos = c - row * 12;
        woff[j] = (unsigned)(row * DM * 2 + min(pos, maxc) * 16);
        woffl[j] = (unsigned)(min(row, 31) * DM * 2 + min(pos, maxc) * 16);
    }
    auto issue = [&](int t) {
        const unsigned b = lds0 + (t & 3) * OU_STAGE;
        const char* wb = (const char*)((t < 10 ? wo + (size_t)t * 64 * DM : wout + (size_t)(t - 10) * 64 * DM) + nt * 96);
        glds16(xb + t * 128, xoff, b + w * 1024);
        glds16(wb, t == 23 ? woffl[0] : woff[0], b + 8192 + w * 1024);
        if (w < 4) glds16(wb, t == 23 ? woffl[1] : woff[1], b + 8192 + (w + 8) * 1024);
    };
    issue(0); issue(1); issue(2);
    const int tm = w & 1, tn = w >> 1;
    const unsigned xa = (unsigned)((32 * tm + r32) * 128), xr = (unsigned)((r32 >> 1) & 7);
    const unsigned wa = (unsigned)(8192 + (8 * h + q) * 192 + tn * 64 + ((lane >> 4) & 1) * 32 + p * 8);
    f32x16 acc = {};
#pragma unroll
    for (int t = 0; t < 24; ++t) {
        if (w < 4) { if (t < 22) waitv_bar<6>(); else if (t == 22) waitv_bar<3>(); else waitv_bar<0>(); }
        else { if (t < 22) waitv_bar<4>(); else if (t == 22) waitv_bar<2>(); else waitv_bar<0>(); }
        if (t + 3 < 24) issue(t + 3);
        if (w < 6) {
            const unsigned b = lds0 + (t & 3) * OU_STAGE;
#pragma unroll
            for (int ks = 0; ks < (t == 23 ? 2 : 4); ++ks) {
                const bf16x8 xf = lds_b128(b + xa + ((((unsigned)(2 * ks + h)) ^ xr) << 4));
                const unsigned a = b + wa + ks * 16 * 192;
                acc = mfma32(cat8(lds_tr(a), lds_tr(a + 4 * 192)), xf, acc);
            }
        }
    }
    const int c0 = nt * 96 + 32 * tn;
    if (w < 6 && c0 < DM) {
        u16* op = out + (size_t)(panel * 64 + 32 * tm + r32) * DM + c0 + 4 * h;
#pragma unroll
        for (int gi = 0; gi < 4; ++gi) *(u32x2*)(op + 8 * gi) = pack4(f32x4{acc[4 * gi], acc[4 * gi + 1], acc[4 * gi + 2], acc[4 * gi + 3]});
    }
}

extern "C" void kernel_launch(void* const* d_in, const int* in_sizes, int n_in, void* d_out, int out_size, void* d_ws, size_t ws_size,
                              hipStream_t stream) {
    const u16* x = (const u16*)d_in[0];
    const u16* wq = (const u16*)d_in[1];
    const u16* wk = (const u16*)d_in[2];
    const u16* wv = (const u16*)d_in[3];
    const u16* wo = (const u16*)d_in[4];
    const u16* win = (const u16*)d_in[6];
    const u16* wsp = (const u16*)d_in[7];
    const u16* bsp = (const u16*)d_in[8];
    const u16* wout = (const u16*)d_in[9];
    const u16* gw = (const u16*)d_in[10];
    char* ws = (char*)d_ws;
    (void)hipFuncSetAttribute((const void*)k1_proj, hipFuncAttributeMaxDynamicSharedMemorySize, K1_LDS);
    (void)hipFuncSetAttribute((const void*)k_attn, hipFuncAttributeMaxDynamicSharedMemorySize, AT_LDS);
    (void)hipFuncSetAttribute((const void*)k_sp, hipFuncAttributeMaxDynamicSharedMemorySize, SP_LDS);
    (void)hipFuncSetAttribute((const void*)k_out, hipFuncAttributeMaxDynamicSharedMemorySize, OU_LDS);
    k1_proj<<<256, 256, K1_LDS, stream>>>(x, wq, wk, wv, win, gw, ws);
    k_ln<<<256, 512, 0, stream>>>(ws);
    k_attn<<<256, 512, AT_LDS, stream>>>(ws);
    k_sp<<<224, 512, SP_LDS, stream>>>(wsp, bsp, ws);
    k_out<<<224, 512, OU_LDS, stream>>>(wo, wout, ws, (u16*)d_out);
}
```

```cpp
#include <hip/hip_runtime.h>
#include <hip/hip_bf16.h>
#include <stdint.h>

typedef __attribute__((ext_vector_type(8))) __bf16 bf16x8;
typedef __attribute__((ext_vector_type(4))) __bf16 bf16x4;
typedef __attribute__((ext_vector_type(4))) short s16x4;
typedef __attribute__((ext_vector_type(8))) short s16x8;
typedef __attribute__((ext_vector_type(4))) float f32x4;
typedef __attribute__((ext_vector_type(8))) float f32x8;
typedef __attribute__((ext_vector_type(16))) float f32x16;
typedef __attribute__((ext_vector_type(2))) unsigned u32x2;
typedef __attribute__((ext_vector_type(4))) unsigned u32x4;
typedef unsigned short u16;
#define LDS __attribute__((address_space(3)))
#define DI __device__ __forceinline__

constexpr int SEQ = 2048, DM = 640, FH = 864, F2 = 1728;
constexpr int ZLD = 896;
constexpr int A3LD = 1536;
constexpr int KVT = 3072;
constexpr size_t WS_Q = 0;
constexpr size_t WS_Z1 = WS_Q + (size_t)SEQ * DM * 2;
constexpr size_t WS_Z2 = WS_Z1 + (size_t)SEQ * ZLD * 2;
constexpr size_t WS_ZN = WS_Z2 + (size_t)SEQ * ZLD * 2;
constexpr size_t WS_A3 = WS_ZN + (size_t)SEQ * ZLD * 2;
constexpr size_t WS_KV = WS_A3 + (size_t)SEQ * A3LD * 2;
constexpr size_t WS_G = WS_KV + 64 * KVT;
constexpr float QSCALE = 0.22360679774997896f * 1.4426950408889634f;

constexpr size_t WS_DBG = 64u<<20;
struct Stamp { unsigned long long c,r; };
DI Stamp stamp(){ Stamp s; asm volatile("s_memtime %0\n\ts_memrealtime %1\n\ts_waitcnt lgkmcnt(0)" : "=s"(s.c), "=s"(s.r) :: "memory"); return s; }
DI void dump(char* ws,int kid,Stamp a,Stamp b){ if(threadIdx.x==0){ unsigned long long* p=(unsigned long long*)(ws+WS_DBG)+((size_t)kid*512+blockIdx.x)*4; p[0]=a.c;p[1]=a.r;p[2]=b.c;p[3]=b.r; } }
DI void glds16(const void* sbase, unsigned voff, unsigned lds_dst) {
    asm volatile("s_mov_b32 m0, %2\n\ts_nop 0\n\tglobal_load_lds_dwordx4 %0, %1" ::"v"(voff), "s"(sbase), "s"(lds_dst) : "memory");
}
#define WAITV_BAR(N) asm volatile("s_waitcnt vmcnt(" #N ") lgkmcnt(0)\n\ts_barrier" ::: "memory")
template <int N> DI void waitv_bar() { asm volatile("s_waitcnt vmcnt(%0) lgkmcnt(0)\n\ts_barrier" ::"i"(N) : "memory"); }

DI bf16x8 lds_b128(unsigned a) { return *(const LDS bf16x8*)(a); }
DI s16x4 lds_b64(unsigned a) { return *(const LDS s16x4*)(a); }
DI s16x4 lds_tr(unsigned a) { return __builtin_amdgcn_ds_read_tr16_b64_v4i16((LDS s16x4*)(a)); }
DI bf16x8 cat8(s16x4 lo, s16x4 hi) { return __builtin_bit_cast(bf16x8, __builtin_shufflevector(lo, hi, 0, 1, 2, 3, 4, 5, 6, 7)); }
DI f32x4 mfma16(bf16x8 a, bf16x8 b, f32x4 c) { return __builtin_amdgcn_mfma_f32_16x16x32_bf16(a, b, c, 0, 0, 0); }
DI f32x16 mfma32(bf16x8 a, bf16x8 b, f32x16 c) { return __builtin_amdgcn_mfma_f32_32x32x16_bf16(a, b, c, 0, 0, 0); }
DI f32x16 mfma32k8(s16x4 a, s16x4 b, f32x16 c) { return __builtin_amdgcn_mfma_f32_32x32x8bf16_1k(a, b, c, 0, 0, 0); }
DI u32x2 pack4(f32x4 v) { return __builtin_bit_cast(u32x2, __builtin_convertvector(v, bf16x4)); }
DI float bf2f(u16 v) { return __uint_as_float((unsigned)v << 16); }
DI float bflo(unsigned v) { return __uint_as_float(v << 16); }
DI float bfhi(unsigned v) { return __uint_as_float(v & 0xffff0000u); }
DI float gelu_tanh(float x) {
    const float c0 = -2.0f * 0.7978845608028654f * 1.4426950408889634f, c1 = c0 * 0.044715f;
    const float e = __builtin_amdgcn_exp2f(x * __builtin_fmaf(x * x, c1, c0));
    return x * __builtin_amdgcn_rcpf(1.0f + e);
}

constexpr int K1_STAGE = 36864, K1_LDS = 4 * K1_STAGE;

__global__ __launch_bounds__(256, 1) void k1_proj(const u16* __restrict__ x, const u16* __restrict__ wq, const u16* __restrict__ wk,
                                                   const u16* __restrict__ wv, const u16* __restrict__ win, const u16* __restrict__ gw,
                                                   char* __restrict__ ws) {
    extern __shared__ __attribute__((aligned(16))) char smem[];
    Stamp s0=stamp();
    const unsigned lds0 = (unsigned)(uintptr_t)smem;
    const int tid = threadIdx.x, lane = tid & 63, w = __builtin_amdgcn_readfirstlane(tid >> 6);
    const int bid = blockIdx.x, xcd = bid & 7, idx = bid >> 3;
    const int mt = (xcd & 1) * 8 + (idx & 7), nt = (xcd >> 1) * 4 + (idx >> 3);
    const int l15 = lane & 15, g = lane >> 4, q = l15 >> 2, p = lane & 3;
    const char* xb = (const char*)(x + (size_t)mt * 128 * DM);
    const unsigned xoff = (unsigned)((8 * w + (lane >> 3)) * DM * 2 + (((lane & 7) ^ ((4 * w + (lane >> 4)) & 7)) << 4));
    const unsigned xr = (unsigned)(l15 >> 1);

    if (nt < 15) {
        const int wm = w & 1, wn = w >> 1;
        const int ldw = nt < 4 ? DM : F2;
        const char* wb = (const char*)(nt < 4 ? wq + nt * 160 : win + (nt - 4) * 160);
        const int maxc = nt == 14 ? 15 : 19;
        unsigned woff[5];
#pragma unroll
        for (int j = 0; j < 5; ++j) {
            const int c = (w + 4 * j) * 64 + lane, row = c / 20, pos = c - row * 20;
            const int sc = min(pos ^ (((row >> 3) & 1) << 1), maxc);
            woff[j] = (unsigned)(row * ldw * 2 + sc * 16);
        }
        auto issue = [&](int t) {
            const unsigned b = lds0 + (t & 3) * K1_STAGE;
#pragma unroll
            for (int j = 0; j < 4; ++j) glds16(xb + t * 128 + j * 32 * DM * 2, xoff, b + (w + 4 * j) * 1024);
#pragma unroll
            for (int j = 0; j < 5; ++j) glds16(wb + (size_t)t * 64 * ldw * 2, woff[j], b + 16384 + (w + 4 * j) * 1024);
        };
        f32x4 acc[4][5];
#pragma unroll
        for (int a = 0; a < 4; ++a)
#pragma unroll
            for (int b = 0; b < 5; ++b) acc[a][b] = f32x4{0.f, 0.f, 0.f, 0.f};
        issue(0); issue(1); issue(2);
        const unsigned xa = (unsigned)((64 * wm + l15) * 128);
        const unsigned wa = (unsigned)(16384 + (8 * g + q) * 320 + p * 8);
#pragma unroll
        for (int t = 0; t < 10; ++t) {
            if (t < 8) waitv_bar<18>(); else if (t == 8) waitv_bar<9>(); else waitv_bar<0>();
            if (t + 3 < 10) issue(t + 3);
            const unsigned b = lds0 + (t & 3) * K1_STAGE;
#pragma unroll
            for (int ks = 0; ks < 2; ++ks) {
                bf16x8 xf[4], wf[5];
#pragma unroll
                for (int mi = 0; mi < 4; ++mi) xf[mi] = lds_b128(b + xa + mi * 2048 + ((((unsigned)(4 * ks + g)) ^ xr) << 4));
#pragma unroll
                for (int ni = 0; ni < 5; ++ni) {
                    const unsigned a = b + wa + ks * 32 * 320 + (unsigned)(((5 * wn + ni) ^ (g & 1)) * 32);
                    wf[ni] = cat8(lds_tr(a), lds_tr(a + 4 * 320));
                }
#pragma unroll
                for (int mi = 0; mi < 4; ++mi)
#pragma unroll
                    for (int ni = 0; ni < 5; ++ni) acc[mi][ni] = mfma16(wf[ni], xf[mi], acc[mi][ni]);
            }
        }
        const int tok0 = mt * 128 + 64 * wm + l15;
        if (nt < 4) {
            u16* qo = (u16*)(ws + WS_Q);
#pragma unroll
            for (int mi = 0; mi < 4; ++mi)
#pragma unroll
                for (int ni = 0; ni < 5; ++ni)
                    *(u32x2*)(qo + (size_t)(tok0 + 16 * mi) * DM + nt * 160 + wn * 80 + ni * 16 + 4 * g) = pack4(acc[mi][ni] * QSCALE);
        } else {
#pragma unroll
            for (int ni = 0; ni < 5; ++ni) {
                const int c0 = (nt - 4) * 160 + wn * 80 + ni * 16;
                if (c0 < F2) {
                    u16* zo = (u16*)(ws + (c0 < FH ? WS_Z1 : WS_Z2)) + (c0 < FH ? c0 : c0 - FH) + 4 * g;
#pragma unroll
                    for (int mi = 0; mi < 4; ++mi) {
                        f32x4 v = acc[mi][ni];
#pragma unroll
                        for (int r = 0; r < 4; ++r) v[r] = gelu_tanh(v[r]);
                        *(u32x2*)(zo + (size_t)(tok0 + 16 * mi) * ZLD) = pack4(v);
                    }
                }
            }
        }
    } else {
        const unsigned img = lds0 + 65536;
        auto issue = [&](int t) {
            const unsigned b = lds0 + (t & 3) * 16384;
#pragma unroll
            for (int j = 0; j < 4; ++j) glds16(xb + t * 128 + j * 32 * DM * 2, xoff, b + (w + 4 * j) * 1024);
        };
        {
            u32x2 tk[13], tv[13];
#pragma unroll
            for (int j = 0; j < 13; ++j) {
                const int c = min(tid + 256 * j, 3199);
                tk[j] = *(const u32x2*)(wk + c * 4);
                tv[j] = *(const u32x2*)(wv + c * 4);
            }
            u16 tg[3];
#pragma unroll
            for (int j = 0; j < 3; ++j) tg[j] = gw[min(tid + 256 * j, 639)];
#pragma unroll
            for (int j = 0; j < 13; ++j) {
                const int c = min(tid + 256 * j, 3199), row = c / 5, pc = c - row * 5;
                const int xs = ((row >> 1) & 1) | (((row >> 3) & 1) << 1);
                const int blk = pc >> 2, po = (pc & 3) * 8;
                *(LDS u32x2*)(img + row * 128 + ((blk ^ xs) * 32) + po) = tk[j];
                *(LDS u32x2*)(img + row * 128 + (((2 + blk) ^ xs) * 32) + po) = tv[j];
            }
#pragma unroll
            for (int j = 0; j < 3; ++j) {
                const int row = min(tid + 256 * j, 639);
                const int xs = ((row >> 1) & 1) | (((row >> 3) & 1) << 1);
                *(LDS u16*)(img + row * 128 + ((1 ^ xs) * 32) + 8) = tg[j];
            }
        }
        asm volatile("s_waitcnt vmcnt(0)" ::: "memory");
        issue(0); issue(1); issue(2);
        f32x4 acc[2][4];
#pragma unroll
        for (int a = 0; a < 2; ++a)
#pragma unroll
            for (int b = 0; b < 4; ++b) acc[a][b] = f32x4{0.f, 0.f, 0.f, 0.f};
        const unsigned xa = (unsigned)((32 * w + l15) * 128);
        const unsigned xs = (unsigned)(((q >> 1) & 1) | ((g & 1) << 1));
        const unsigned wa = img + (unsigned)((8 * g + q) * 128 + p * 8);
#pragma unroll
        for (int t = 0; t < 10; ++t) {
            if (t < 8) waitv_bar<8>(); else if (t == 8) waitv_bar<4>(); else waitv_bar<0>();
            if (t + 3 < 10) issue(t + 3);
            const unsigned b = lds0 + (t & 3) * 16384;
#pragma unroll
            for (int ks = 0; ks < 2; ++ks) {
                bf16x8 xf[2], wf[4];
#pragma unroll
                for (int mi = 0; mi < 2; ++mi) xf[mi] = lds_b128(b + xa + mi * 2048 + ((((unsigned)(4 * ks + g)) ^ xr) << 4));
#pragma unroll
                for (int ni = 0; ni < 4; ++ni) {
                    const unsigned a = wa + (t * 64 + ks * 32) * 128 + (((unsigned)ni ^ xs) * 32);
                    wf[ni] = cat8(lds_tr(a), lds_tr(a + 4 * 128));
                }
#pragma unroll
                for (int mi = 0; mi < 2; ++mi) {
                    acc[mi][0] = mfma16(wf[0], xf[mi], acc[mi][0]);
                    acc[mi][1] = mfma16(wf[1], xf[mi], acc[mi][1]);
                    acc[mi][2] = mfma16(xf[mi], wf[2], acc[mi][2]);
                    acc[mi][3] = mfma16(xf[mi], wf[3], acc[mi][3]);
                }
            }
        }
        char* kv = ws + WS_KV + (size_t)(mt * 4 + w) * KVT;
        float* go = (float*)(ws + WS_G) + mt * 128 + 32 * w;
#pragma unroll
        for (int mi = 0; mi < 2; ++mi) {
            const int key = 16 * mi + l15;
            *(u32x2*)(kv + (key + 32 * (g >> 1)) * 16 + (g & 1) * 8) = pack4(acc[mi][0]);
            if (g == 0) {
                *(u32x2*)(kv + 1024 + key * 8) = pack4(acc[mi][1]);
                *(u32x2*)(kv + 1024 + 256 + key * 8) = u32x2{0x00003f80u, 0u};
            }
            if (g == 1) go[key] = __builtin_amdgcn_rcpf(1.0f + __builtin_amdgcn_exp2f(-1.4426950408889634f * acc[mi][1][0]));
            char* vb = kv + 1536 + mi * 768 + (g & 1) * 384 + (g >> 1) * 8;
            *(u32x2*)(vb + l15 * 16) = pack4(acc[mi][2]);
            if (l15 < 8) {
                u32x2 val = pack4(acc[mi][3]);
                if (l15 == 4) val = u32x2{0x3f803f80u, 0x3f803f80u};
                if (l15 > 4) val = u32x2{0u, 0u};
                *(u32x2*)(vb + (16 + l15) * 16) = val;
            }
        }
    }
    asm volatile("s_waitcnt vmcnt(0)":::"memory"); dump(ws,0,s0,stamp());
}

DI float wave_sum(float v) {
#pragma unroll
    for (int o = 32; o > 0; o >>= 1) v += __shfl_xor(v, o);
    return v;
}
DI void ln_row(const char* ws_z2, char* ws_zn, int row, int lane) {
    const u32x4* src = (const u32x4*)(ws_z2 + (size_t)row * ZLD * 2);
    const bool act = lane < 54;
    u32x4 a = u32x4{0u, 0u, 0u, 0u}, b = a;
    if (act) { a = src[2 * lane]; b = src[2 * lane + 1]; }
    float v[16];
#pragma unroll
    for (int i = 0; i < 4; ++i) { v[2 * i] = bflo(a[i]); v[2 * i + 1] = bfhi(a[i]); v[8 + 2 * i] = bflo(b[i]); v[9 + 2 * i] = bfhi(b[i]); }
    float s = 0.f;
#pragma unroll
    for (int i = 0; i < 16; ++i) s += v[i];
    const float mean = wave_sum(s) * (1.0f / FH);
    float ss = 0.f;
#pragma unroll
    for (int i = 0; i < 16; ++i) { v[i] = act ? v[i] - mean : 0.f; ss += v[i] * v[i]; }
    const float rstd = __builtin_amdgcn_rsqf(wave_sum(ss) * (1.0f / FH) + 1e-5f);
    f32x8 lo, hi;
#pragma unroll
    for (int i = 0; i < 8; ++i) { lo[i] = v[i] * rstd; hi[i] = v[8 + i] * rstd; }
    if (lane < 56) {
        bf16x8* dst = (bf16x8*)(ws_zn + (size_t)row * ZLD * 2);
        dst[2 * lane] = __builtin_convertvector(lo, bf16x8);
        dst[2 * lane + 1] = __builtin_convertvector(hi, bf16x8);
    }
}
__global__ __launch_bounds__(512) void k_ln(char* __restrict__ ws) {
    Stamp s0=stamp();
    ln_row(ws + WS_Z2, ws + WS_ZN, blockIdx.x * 8 + (threadIdx.x >> 6), threadIdx.x & 63);
    asm volatile("s_waitcnt vmcnt(0)":::"memory"); dump(ws,1,s0,stamp());
}

constexpr int AT_STAGE = 8 * KVT, AT_LDS = 3 * AT_STAGE;

DI float half_max(float m) {
    auto rr = __builtin_amdgcn_permlane32_swap(__float_as_uint(m), __float_as_uint(m), false, false);
    return __builtin_fmaxf(__uint_as_float(rr[0]), __uint_as_float(rr[1]));
}
DI float max16(const f32x16& s) {
    float a = __builtin_fmaxf(s[0], s[1]);
#pragma unroll
    for (int i = 2; i < 16; ++i) a = __builtin_fmaxf(a, s[i]);
    return a;
}
DI void exp_pack(const f32x16& s, bf16x8& p0, bf16x8& p1) {
    f32x8 lo, hi;
#pragma unroll
    for (int i = 0; i < 8; ++i) { lo[i] = __builtin_amdgcn_exp2f(s[i]); hi[i] = __builtin_amdgcn_exp2f(s[8 + i]); }
    p0 = __builtin_convertvector(lo, bf16x8);
    p1 = __builtin_convertvector(hi, bf16x8);
}
DI f32x16 attn_exact(const char* kv, bf16x8 q0, s16x4 q1, int lane) {
    const int r32 = lane & 31, h = lane >> 5;
    if (h) q1 = s16x4{0, 0, 0, 0};
    f32x16 o = {};
    float m = -INFINITY;
    const unsigned vo = 1536 + h * 384 + min(r32, 23) * 16;
    for (int t = 0; t < 64; ++t) {
        const char* b = kv + t * KVT;
        const bf16x8 k0 = *(const bf16x8*)(b + lane * 16);
        const s16x4 k1 = *(const s16x4*)(b + 1024 + lane * 8);
        const bf16x8 v0 = *(const bf16x8*)(b + vo), v1 = *(const bf16x8*)(b + vo + 768);
        f32x16 s = mfma32k8(k1, q1, mfma32(k0, q0, f32x16{}));
        const float mn = __builtin_fmaxf(m, half_max(max16(s)));
        const float al = __builtin_amdgcn_exp2f(m - mn);
        m = mn;
#pragma unroll
        for (int i = 0; i < 16; ++i) { s[i] -= mn; o[i] *= al; }
        bf16x8 p0, p1;
        exp_pack(s, p0, p1);
        o = mfma32(v1, p1, mfma32(v0, p0, o));
    }
    return o;
}

DI void attn_body(char* __restrict__ ws, unsigned lds0, int panel, int hg) {
    const int tid = threadIdx.x, lane = tid & 63, w = __builtin_amdgcn_readfirstlane(tid >> 6);
    const int r32 = lane & 31, h = lane >> 5, kh = w >> 2, head = hg * 4 + (w & 3);
    const char* kv = ws + WS_KV;
    const char* src = kv + (size_t)((w >> 2) * 32 + (w & 3)) * KVT;
    auto issue = [&](int t, unsigned buf) {
#pragma unroll
        for (int j = 0; j < 3; ++j) glds16(src + (size_t)t * 4 * KVT + j * 1024, (unsigned)lane * 16, buf + w * KVT + j * 1024);
    };
    issue(0, lds0);
    const u16* qp = (const u16*)(ws + WS_Q) + (size_t)(panel * 64 + r32) * DM + head * 20;
    const bf16x8 qA0 = *(const bf16x8*)(qp + 8 * h), qB0 = *(const bf16x8*)(qp + 32 * DM + 8 * h);
    s16x4 qA1 = *(const s16x4*)(qp + 16), qB1 = *(const s16x4*)(qp + 32 * DM + 16);
    const float gA = ((const float*)(ws + WS_G))[panel * 64 + 32 * kh + r32];
    asm volatile("s_waitcnt vmcnt(0)" ::: "memory");
    issue(1, lds0 + AT_STAGE);
    if (h) { qA1 = s16x4{0, 0, 0, 0}; qB1 = s16x4{0, 0, 0, 0}; }
    waitv_bar<3>();
    const unsigned ko = lane * 16, k1o = 1024 + lane * 8, vo = 1536 + h * 384 + min(r32, 23) * 16;
    {
        const bf16x8 k0 = lds_b128(lds0 + ko);
        const s16x4 k1 = lds_b64(lds0 + k1o);
        const float mA = half_max(max16(mfma32k8(k1, qA1, mfma32(k0, qA0, f32x16{}))));
        const float mB = half_max(max16(mfma32k8(k1, qB1, mfma32(k0, qB0, f32x16{}))));
        if (h) {
            qA1[0] = (short)(__float_as_uint(-mA) >> 16);
            qB1[0] = (short)(__float_as_uint(-mB) >> 16);
        }
    }
    f32x16 oA = {}, oB = {};
    unsigned cur = lds0, nxt = lds0 + 2 * AT_STAGE;
    for (int t = 0; t < 8; ++t) {
        if (t > 0) { if (t < 7) waitv_bar<3>(); else waitv_bar<0>(); }
        if (t + 2 < 8) issue(t + 2, nxt);
        const unsigned b = cur + kh * 4 * KVT;
#pragma unroll
        for (int kt = 0; kt < 4; ++kt) {
            const bf16x8 k0 = lds_b128(b + kt * KVT + ko);
            const s16x4 k1 = lds_b64(b + kt * KVT + k1o);
            const bf16x8 v0 = lds_b128(b + kt * KVT + vo), v1 = lds_b128(b + kt * KVT + vo + 768);
            const f32x16 sA = mfma32k8(k1, qA1, mfma32(k0, qA0, f32x16{}));
            const f32x16 sB = mfma32k8(k1, qB1, mfma32(k0, qB0, f32x16{}));
            bf16x8 p0, p1;
            exp_pack(sA, p0, p1);
            oA = mfma32(v1, p1, mfma32(v0, p0, oA));
            exp_pack(sB, p0, p1);
            oB = mfma32(v1, p1, mfma32(v0, p0, oB));
        }
        nxt = cur;
        cur = (cur == lds0 + 2 * AT_STAGE) ? lds0 : cur + AT_STAGE;
    }
    asm volatile("s_waitcnt lgkmcnt(0)\n\ts_barrier" ::: "memory");
    {
        const f32x16 snd = kh ? oA : oB;
        const unsigned a = lds0 + w * 3072 + lane * 16;
#pragma unroll
        for (int i = 0; i < 3; ++i) *(LDS f32x4*)(a + i * 1024) = f32x4{snd[4 * i], snd[4 * i + 1], snd[4 * i + 2], snd[4 * i + 3]};
    }
    asm volatile("s_waitcnt lgkmcnt(0)\n\ts_barrier" ::: "memory");
    f32x16 o = kh ? oB : oA;
    {
        const unsigned a = lds0 + (w ^ 4) * 3072 + lane * 16;
#pragma unroll
        for (int i = 0; i < 3; ++i) {
            const f32x4 r = *(const LDS f32x4*)(a + i * 1024);
#pragma unroll
            for (int j = 0; j < 4; ++j) o[4 * i + j] += r[j];
        }
    }
    {
        float mx = 0.f;
#pragma unroll
        for (int i = 0; i < 12; ++i) mx = __builtin_fmaxf(mx, __builtin_fabsf(o[i]));
        bool bad = false;
#pragma unroll
        for (int i = 0; i < 12; ++i) bad |= !(__builtin_fabsf(o[i]) < 1e30f);
        if (__any(bad)) o = attn_exact(kv, kh ? qB0 : qA0, kh ? qB1 : qA1, lane);
    }
    const float lsum = __shfl(o[8], r32 + 32);
    const float sc = gA * __builtin_amdgcn_rcpf(lsum);
    u16* op = (u16*)(ws + WS_A3) + (size_t)(panel * 64 + 32 * kh + r32) * A3LD + head * 20 + 4 * h;
    *(u32x2*)(op) = pack4(f32x4{o[0], o[1], o[2], o[3]} * sc);
    *(u32x2*)(op + 8) = pack4(f32x4{o[4], o[5], o[6], o[7]} * sc);
    if (h == 0) *(u32x2*)(op + 16) = pack4(f32x4{o[8], o[9], o[10], o[11]} * sc);
}
__global__ __launch_bounds__(512, 4) void k_attn(char* __restrict__ ws) {
    extern __shared__ __attribute__((aligned(16))) char smem[];
    const int bid = blockIdx.x;
    Stamp s0=stamp();
    attn_body(ws, (unsigned)(uintptr_t)smem, (bid & 7) * 4 + (bid >> 6), (bid >> 3) & 7);
    asm volatile("s_waitcnt vmcnt(0)":::"memory"); dump(ws,2,s0,stamp());
}

constexpr int SP_STAGE = 24576, SP_LDS = 3 * SP_STAGE;
DI void sp_body(const u16* __restrict__ wsp, const u16* __restrict__ bsp, char* __restrict__ ws, unsigned lds0, int panel, int nt) {
    const int tid = threadIdx.x, lane = tid & 63, w = __builtin_amdgcn_readfirstlane(tid >> 6);
    const int r32 = lane & 31, h = lane >> 5, q = (lane & 15) >> 2, p = lane & 3;
    const char* xb = (const char*)(wsp + (size_t)(panel * 64 + 8 * w) * SEQ);
    const unsigned xoff = (unsigned)((lane >> 3) * SEQ * 2 + (((lane & 7) ^ ((4 * w + (lane >> 4)) & 7)) << 4));
    const char* wb = ws + WS_ZN + (size_t)(8 * w) * ZLD * 2 + nt * 256;
    const unsigned woff = (unsigned)((lane >> 4) * ZLD * 2 + (((lane & 15) ^ ((lane >> 4) << 2)) << 4));
    auto issue = [&](int t, unsigned buf) {
        glds16(xb + t * 128, xoff, buf + w * 1024);
        glds16(wb + (size_t)t * 64 * ZLD * 2, woff, buf + 8192 + w * 2048);
        glds16(wb + (size_t)(t * 64 + 4) * ZLD * 2, woff, buf + 8192 + w * 2048 + 1024);
    };
    issue(0, lds0);
    issue(1, lds0 + SP_STAGE);
    const int tm = w & 1, tn = w >> 1;
    const unsigned xa = (unsigned)((32 * tm + r32) * 128), xr = (unsigned)((r32 >> 1) & 7);
    const unsigned wa = (unsigned)(8192 + (8 * h + q) * 256 + (((4 * tn + 2 * ((lane >> 4) & 1) + (p >> 1)) ^ (q << 2)) << 4) + (p & 1) * 8);
    f32x16 acc = {};
    unsigned cur = lds0, nxt = lds0 + 2 * SP_STAGE;
    for (int t = 0; t < 32; ++t) {
        if (t < 31) waitv_bar<3>(); else waitv_bar<0>();
        if (t + 2 < 32) issue(t + 2, nxt);
#pragma unroll
        for (int ks = 0; ks < 4; ++ks) {
            const bf16x8 xf = lds_b128(cur + xa + ((((unsigned)(2 * ks + h)) ^ xr) << 4));
            const unsigned a = cur + wa + ks * 16 * 256;
            acc = mfma32(cat8(lds_tr(a), lds_tr(a + 4 * 256)), xf, acc);
        }
        nxt = cur;
        cur = (cur == lds0 + 2 * SP_STAGE) ? lds0 : cur + SP_STAGE;
    }
    const int tok = panel * 64 + 32 * tm + r32;
    const float bb = bf2f(bsp[tok]), og = 1.0f - ((const float*)(ws + WS_G))[tok];
    const int f0 = nt * 128 + 32 * tn;
    if (f0 < FH) {
        const u16* z1 = (const u16*)(ws + WS_Z1) + (size_t)tok * ZLD + f0 + 4 * h;
        u16* uo = (u16*)(ws + WS_A3) + (size_t)tok * A3LD + DM + f0 + 4 * h;
#pragma unroll
        for (int gi = 0; gi < 4; ++gi) {
            const u32x2 z = *(const u32x2*)(z1 + 8 * gi);
            const f32x4 v = {(acc[4 * gi] + bb) * bflo(z[0]) * og, (acc[4 * gi + 1] + bb) * bfhi(z[0]) * og,
                             (acc[4 * gi + 2] + bb) * bflo(z[1]) * og, (acc[4 * gi + 3] + bb) * bfhi(z[1]) * og};
            *(u32x2*)(uo + 8 * gi) = pack4(v);
        }
    }
}
__global__ __launch_bounds__(512, 4) void k_sp(const u16* __restrict__ wsp, const u16* __restrict__ bsp, char* __restrict__ ws) {
    extern __shared__ __attribute__((aligned(16))) char smem[];
    const int bid = blockIdx.x, idx = bid >> 3;
    Stamp s0=stamp();
    sp_body(wsp, bsp, ws, (unsigned)(uintptr_t)smem, (bid & 7) * 4 + idx / 7, idx % 7);
    asm volatile("s_waitcnt vmcnt(0)":::"memory"); dump(ws,3,s0,stamp());
}

constexpr int OU_STAGE = 20480, OU_LDS = 4 * OU_STAGE;
__global__ __launch_bounds__(512, 2) void k_out(const u16* __restrict__ wo, const u16* __restrict__ wout, const char* __restrict__ ws,
                                                 u16* __restrict__ out) {
    extern __shared__ __attribute__((aligned(16))) char smem[];
    const unsigned lds0 = (unsigned)(uintptr_t)smem;
    Stamp s0=stamp();
    const int bid = blockIdx.x, idx = bid >> 3, panel = (bid & 7) * 4 + idx / 7, nt = idx % 7;
    const int tid = threadIdx.x, lane = tid & 63, w = __builtin_amdgcn_readfirstlane(tid >> 6);
    const int r32 = lane & 31, h = lane >> 5, q = (lane & 15) >> 2, p = lane & 3;
    const char* xb = ws + WS_A3 + (size_t)(panel * 64 + 8 * w) * A3LD * 2;
    const unsigned xoff = (unsigned)((lane >> 3) * A3LD * 2 + (((lane & 7) ^ ((4 * w + (lane >> 4)) & 7)) << 4));
    const int maxc = nt == 6 ? 7 : 11;
    unsigned woff[2], woffl[2];
#pragma unroll
    for (int j = 0; j < 2; ++j) {
        const int c = (w + 8 * j) * 64 + lane, row = c / 12, pos = c - row * 12;
        woff[j] = (unsigned)(row * DM * 2 + min(pos, maxc) * 16);
        woffl[j] = (unsigned)(min(row, 31) * DM * 2 + min(pos, maxc) * 16);
    }
    auto issue = [&](int t) {
        const unsigned b = lds0 + (t & 3) * OU_STAGE;
        const char* wb = (const char*)((t < 10 ? wo + (size_t)t * 64 * DM : wout + (size_t)(t - 10) * 64 * DM) + nt * 96);
        glds16(xb + t * 128, xoff, b + w * 1024);
        glds16(wb, t == 23 ? woffl[0] : woff[0], b + 8192 + w * 1024);
        if (w < 4) glds16(wb, t == 23 ? woffl[1] : woff[1], b + 8192 + (w + 8) * 1024);
    };
    issue(0); issue(1); issue(2);
    const int tm = w & 1, tn = w >> 1;
    const unsigned xa = (unsigned)((32 * tm + r32) * 128), xr = (unsigned)((r32 >> 1) & 7);
    const unsigned wa = (unsigned)(8192 + (8 * h + q) * 192 + tn * 64 + ((lane >> 4) & 1) * 32 + p * 8);
    f32x16 acc = {};
#pragma unroll
    for (int t = 0; t < 24; ++t) {
        if (w < 4) { if (t < 22) waitv_bar<6>(); else if (t == 22) waitv_bar<3>(); else waitv_bar<0>(); }
        else { if (t < 22) waitv_bar<4>(); else if (t == 22) waitv_bar<2>(); else waitv_bar<0>(); }
        if (t + 3 < 24) issue(t + 3);
        if (w < 6) {
            const unsigned b = lds0 + (t & 3) * OU_STAGE;
#pragma unroll
            for (int ks = 0; ks < (t == 23 ? 2 : 4); ++ks) {
                const bf16x8 xf = lds_b128(b + xa + ((((unsigned)(2 * ks + h)) ^ xr) << 4));
                const unsigned a = b + wa + ks * 16 * 192;
                acc = mfma32(cat8(lds_tr(a), lds_tr(a + 4 * 192)), xf, acc);
            }
        }
    }
    const int c0 = nt * 96 + 32 * tn;
    if (w < 6 && c0 < DM) {
        u16* op = out + (size_t)(panel * 64 + 32 * tm + r32) * DM + c0 + 4 * h;
#pragma unroll
        for (int gi = 0; gi < 4; ++gi) *(u32x2*)(op + 8 * gi) = pack4(f32x4{acc[4 * gi], acc[4 * gi + 1], acc[4 * gi + 2], acc[4 * gi + 3]});
    }
    asm volatile("s_waitcnt vmcnt(0)":::"memory"); dump((char*)ws,4,s0,stamp());
}

__global__ void k_spin(char* ws) {
    unsigned long long m = ~0ull;
    for (int i = 0; i < 224; ++i) { unsigned long long v = ((unsigned long long*)(ws + WS_DBG))[(0 * 512 + i) * 4 + 1]; m = v < m ? v : m; }
    for (int it = 0; it < 2000000; ++it) { if (__builtin_amdgcn_s_memrealtime() >= m + 20000ull) break; __builtin_amdgcn_s_sleep(1); }
}
extern "C" void kernel_launch(void* const* d_in, const int* in_sizes, int n_in, void* d_out, int out_size, void* d_ws, size_t ws_size,
                              hipStream_t stream) {
    const u16* x = (const u16*)d_in[0];
    const u16* wq = (const u16*)d_in[1];
    const u16* wk = (const u16*)d_in[2];
    const u16* wv = (const u16*)d_in[3];
    const u16* wo = (const u16*)d_in[4];
    const u16* win = (const u16*)d_in[6];
    const u16* wsp = (const u16*)d_in[7];
    const u16* bsp = (const u16*)d_in[8];
    const u16* wout = (const u16*)d_in[9];
    const u16* gw = (const u16*)d_in[10];
    char* ws = (char*)d_ws;
    (void)hipFuncSetAttribute((const void*)k1_proj, hipFuncAttributeMaxDynamicSharedMemorySize, K1_LDS);
    (void)hipFuncSetAttribute((const void*)k_attn, hipFuncAttributeMaxDynamicSharedMemorySize, AT_LDS);
    (void)hipFuncSetAttribute((const void*)k_sp, hipFuncAttributeMaxDynamicSharedMemorySize, SP_LDS);
    (void)hipFuncSetAttribute((const void*)k_out, hipFuncAttributeMaxDynamicSharedMemorySize, OU_LDS);
    k1_proj<<<256, 256, K1_LDS, stream>>>(x, wq, wk, wv, win, gw, ws);
    k_ln<<<256, 512, 0, stream>>>(ws);
    k_attn<<<256, 512, AT_LDS, stream>>>(ws);
    k_sp<<<224, 512, SP_LDS, stream>>>(wsp, bsp, ws);
    k_out<<<224, 512, OU_LDS, stream>>>(wo, wout, ws, (u16*)d_out);
    k_spin<<<1, 64, 0, stream>>>(ws);
}
```

```cpp
#include <hip/hip_runtime.h>
#include <hip/hip_bf16.h>
#include <stdint.h>

typedef __attribute__((ext_vector_type(8))) __bf16 bf16x8;
typedef __attribute__((ext_vector_type(4))) __bf16 bf16x4;
typedef __attribute__((ext_vector_type(4))) short s16x4;
typedef __attribute__((ext_vector_type(2))) float f32x2;
typedef __attribute__((ext_vector_type(4))) float f32x4;
typedef __attribute__((ext_vector_type(8))) float f32x8;
typedef __attribute__((ext_vector_type(16))) float f32x16;
typedef __attribute__((ext_vector_type(2))) unsigned u32x2;
typedef __attribute__((ext_vector_type(4))) unsigned u32x4;
typedef unsigned short u16;
typedef unsigned long long u64;
#define LDS __attribute__((address_space(3)))
#define GAS __attribute__((address_space(1)))
#define DI __device__ __forceinline__
#define SB() __builtin_amdgcn_sched_barrier(0)
#define STAMP(i)
#define TL(i)
#define TLDECL
#define TLARG
#define TLPASS(b)
#define TLDUMP(k)

#define PSLEEP 4
constexpr int SEQ = 2048, DM = 640, FH = 864, F2 = 1728;
constexpr int KVT = 3072;
constexpr int OBLOB = 11264;
constexpr size_t WS_E = 0;
constexpr int FS = 128;
constexpr size_t WS_FKV = 1024, WS_FZN = WS_FKV + 32 * FS, WS_FU = WS_FZN + 256 * FS, WS_FO = WS_FU + 256 * FS;
constexpr size_t WS_G = 131072;
constexpr size_t WS_ST = WS_G + 8192;
constexpr size_t WS_KV = WS_ST + 32 * 7 * 2048;
constexpr size_t WS_ZN = WS_KV + 64 * KVT;
constexpr size_t WS_U = WS_ZN + 32 * 7 * 16384;
constexpr size_t WS_O = WS_U + 32 * 7 * 16384;
constexpr int LDS_BYTES = 81920;
constexpr float QSCALE = 0.22360679774997896f * 1.4426950408889634f;

template <bool SC1> DI void glds16(const void* sbase, unsigned voff, unsigned lds_dst) {
    if (SC1) asm volatile("s_mov_b32 m0, %2\n\ts_nop 0\n\tglobal_load_lds_dwordx4 %0, %1 sc1" ::"v"(voff), "s"(sbase), "s"(lds_dst) : "memory");
    else asm volatile("s_mov_b32 m0, %2\n\ts_nop 0\n\tglobal_load_lds_dwordx4 %0, %1" ::"v"(voff), "s"(sbase), "s"(lds_dst) : "memory");
}
template <int N> DI void waitv_bar() { asm volatile("s_waitcnt vmcnt(%0) lgkmcnt(0)\n\ts_barrier" ::"i"(N) : "memory"); }
DI void bar_l() { asm volatile("s_waitcnt lgkmcnt(0)\n\ts_barrier" ::: "memory"); }
DI void st16_wt(void* sbase, unsigned voff, u32x4 v) {
    asm volatile("global_store_dwordx4 %0, %1, %2 sc1\n\ts_nop 1" ::"v"(voff), "v"(v), "s"(sbase) : "memory");
}
DI u32x4 ld16_issue(const void* sbase, unsigned voff) {
    u32x4 v;
    asm volatile("global_load_dwordx4 %0, %1, %2 sc1" : "=v"(v) : "v"(voff), "s"(sbase) : "memory");
    return v;
}
#define WAIT14(p) asm volatile("s_waitcnt vmcnt(0)" : "+v"(p[0]), "+v"(p[1]), "+v"(p[2]), "+v"(p[3]), "+v"(p[4]), "+v"(p[5]), "+v"(p[6]), \
                               "+v"(p[7]), "+v"(p[8]), "+v"(p[9]), "+v"(p[10]), "+v"(p[11]), "+v"(p[12]), "+v"(p[13])::"memory")
#define RLX_AGENT __ATOMIC_RELAXED, __HIP_MEMORY_SCOPE_AGENT
DI u64 ld8_dev(const void* p) { return __hip_atomic_load((GAS u64*)p, RLX_AGENT); }
DI unsigned ld4_dev(const void* p) { return __hip_atomic_load((GAS unsigned*)p, RLX_AGENT); }
DI void st8_dev(void* p, u64 v) { __hip_atomic_store((GAS u64*)p, v, RLX_AGENT); }
DI u64 read_tag(const char* ws) {
    u64 t;
    asm volatile("s_load_dwordx2 %0, %1, 0x0\n\ts_waitcnt lgkmcnt(0)" : "=s"(t) : "s"(ws) : "memory");
    t += 0x9E3779B97F4A7C15ull;
    t = (t ^ (t >> 30)) * 0xBF58476D1CE4E5B9ull;
    t = (t ^ (t >> 27)) * 0x94D049BB133111EBull;
    return t ^ (t >> 31);
}
DI void poll_flags(const char* p, u64 tag) {
#pragma unroll 1
    for (unsigned spins = 0; spins < (1u << 18); ++spins) {
        if (__all(ld8_dev(p) == tag)) break;
        __builtin_amdgcn_s_sleep(PSLEEP);
    }
    __builtin_amdgcn_fence(__ATOMIC_ACQUIRE, "wavefront");
}

DI bf16x8 lds_b128(unsigned a) { return *(const LDS bf16x8*)(a); }
DI u32x4 lds_u128(unsigned a) { return *(const LDS u32x4*)(a); }
DI s16x4 lds_b64(unsigned a) { return *(const LDS s16x4*)(a); }
DI void lds_w64(unsigned a, u32x2 v) { *(LDS u32x2*)(a) = v; }
DI s16x4 lds_tr(unsigned a) { return __builtin_amdgcn_ds_read_tr16_b64_v4i16((LDS s16x4*)(a)); }
DI bf16x8 cat8(s16x4 lo, s16x4 hi) { return __builtin_bit_cast(bf16x8, __builtin_shufflevector(lo, hi, 0, 1, 2, 3, 4, 5, 6, 7)); }
DI f32x4 mfma16(bf16x8 a, bf16x8 b, f32x4 c) { return __builtin_amdgcn_mfma_f32_16x16x32_bf16(a, b, c, 0, 0, 0); }
DI f32x16 mfma32(bf16x8 a, bf16x8 b, f32x16 c) { return __builtin_amdgcn_mfma_f32_32x32x16_bf16(a, b, c, 0, 0, 0); }
DI f32x16 mfma32k8(s16x4 a, s16x4 b, f32x16 c) { return __builtin_amdgcn_mfma_f32_32x32x8bf16_1k(a, b, c, 0, 0, 0); }
DI u32x2 pack4(f32x4 v) { return __builtin_bit_cast(u32x2, __builtin_convertvector(v, bf16x4)); }
DI f32x4 quad(const f32x16& a, int g) { return f32x4{a[4 * g], a[4 * g + 1], a[4 * g + 2], a[4 * g + 3]}; }
DI float bf2f(u16 v) { return __uint_as_float((unsigned)v << 16); }
DI float bflo(unsigned v) { return __uint_as_float(v << 16); }
DI float bfhi(unsigned v) { return __uint_as_float(v & 0xffff0000u); }
DI f32x4 unpack4(u32x2 v) { return f32x4{bflo(v[0]), bfhi(v[0]), bflo(v[1]), bfhi(v[1])}; }
DI float gelu_tanh(float x) {
    const float c0 = -2.0f * 0.7978845608028654f * 1.4426950408889634f, c1 = c0 * 0.044715f;
    const float e = __builtin_amdgcn_exp2f(x * __builtin_fmaf(x * x, c1, c0));
    return x * __builtin_amdgcn_rcpf(1.0f + e);
}
DI u32x2 gelu_pack(f32x4 v) { return pack4(f32x4{gelu_tanh(v[0]), gelu_tanh(v[1]), gelu_tanh(v[2]), gelu_tanh(v[3])}); }
DI float half_sum(float m) {
    auto rr = __builtin_amdgcn_permlane32_swap(__float_as_uint(m), __float_as_uint(m), false, false);
    return __uint_as_float(rr[0]) + __uint_as_float(rr[1]);
}
DI float half_max(float m) {
    auto rr = __builtin_amdgcn_permlane32_swap(__float_as_uint(m), __float_as_uint(m), false, false);
    return __builtin_fmaxf(__uint_as_float(rr[0]), __uint_as_float(rr[1]));
}
DI int olane() { int t; asm volatile("v_mbcnt_lo_u32_b32 %0, -1, 0\n\tv_mbcnt_hi_u32_b32 %0, -1, %0" : "=v"(t)); return t; }
template <int NP> DI void copy_out_wt(char* dst, unsigned lds_src, int w, int lane) {
#pragma unroll
    for (int j = 0; j < (NP + 7) / 8; ++j) {
        const int pc = w + 8 * j;
        if (pc < NP) st16_wt(dst, (unsigned)(pc * 1024 + lane * 16), lds_u128(lds_src + pc * 1024 + lane * 16));
    }
}

#define ASM_E2 \
    "s_barrier\n\t" \
    "ds_read_b64_tr_b16 v[68:69], %[wa] offset:24576\n\t" \
    "ds_read_b64_tr_b16 v[70:71], %[wa] offset:25600\n\t" \
    "ds_read_b64_tr_b16 v[72:73], %[wb] offset:24576\n\t" \
    "ds_read_b64_tr_b16 v[74:75], %[wb] offset:25600\n\t" \
    "ds_read_b128 v[64:67], %[x0] offset:24576\n\t" \
    "ds_read_b64_tr_b16 v[80:81], %[wa] offset:28672\n\t" \
    "ds_read_b64_tr_b16 v[82:83], %[wa] offset:29696\n\t" \
    "ds_read_b64_tr_b16 v[84:85], %[wb] offset:28672\n\t" \
    "ds_read_b64_tr_b16 v[86:87], %[wb] offset:29696\n\t" \
    "ds_read_b128 v[76:79], %[x1] offset:24576\n\t" \
    "ds_read_b64_tr_b16 v[92:93], %[wa] offset:32768\n\t" \
    "ds_read_b64_tr_b16 v[94:95], %[wa] offset:33792\n\t" \
    "ds_read_b64_tr_b16 v[96:97], %[wb] offset:32768\n\t" \
    "ds_read_b64_tr_b16 v[98:99], %[wb] offset:33792\n\t" \
    "ds_read_b128 v[88:91], %[x2] offset:24576\n\t" \
    "ds_read_b64_tr_b16 v[104:105], %[wa] offset:36864\n\t" \
    "ds_read_b64_tr_b16 v[106:107], %[wa] offset:37888\n\t" \
    "ds_read_b64_tr_b16 v[108:109], %[wb] offset:36864\n\t" \
    "ds_read_b64_tr_b16 v[110:111], %[wb] offset:37888\n\t" \
    "ds_read_b128 v[100:103], %[x3] offset:24576\n\t" \
    "1:\n\t" \
    "s_waitcnt lgkmcnt(15)\n\t" \
    "v_mfma_f32_32x32x16_bf16 v[32:47], v[68:71], v[64:67], v[32:47]\n\t" \
    "v_mfma_f32_32x32x16_bf16 v[48:63], v[72:75], v[64:67], v[48:63]\n\t" \
    "s_waitcnt lgkmcnt(0)\n\t" \
    "s_barrier\n\t" \
    "ds_read_b64_tr_b16 v[68:69], %[wa] offset:49152\n\t" \
    "ds_read_b64_tr_b16 v[70:71], %[wa] offset:50176\n\t" \
    "ds_read_b64_tr_b16 v[72:73], %[wb] offset:49152\n\t" \
    "ds_read_b64_tr_b16 v[74:75], %[wb] offset:50176\n\t" \
    "ds_read_b128 v[64:67], %[x0] offset:49152\n\t" \
    "v_mfma_f32_32x32x16_bf16 v[32:47], v[80:83], v[76:79], v[32:47]\n\t" \
    "ds_read_b64_tr_b16 v[80:81], %[wa] offset:53248\n\t" \
    "ds_read_b64_tr_b16 v[82:83], %[wa] offset:54272\n\t" \
    "v_mfma_f32_32x32x16_bf16 v[48:63], v[84:87], v[76:79], v[48:63]\n\t" \
    "ds_read_b64_tr_b16 v[84:85], %[wb] offset:53248\n\t" \
    "ds_read_b64_tr_b16 v[86:87], %[wb] offset:54272\n\t" \
    "ds_read_b128 v[76:79], %[x1] offset:49152\n\t" \
    "v_mfma_f32_32x32x16_bf16 v[32:47], v[92:95], v[88:91], v[32:47]\n\t" \
    "ds_read_b64_tr_b16 v[92:93], %[wa] offset:57344\n\t" \
    "ds_read_b64_tr_b16 v[94:95], %[wa] offset:58368\n\t" \
    "v_mfma_f32_32x32x16_bf16 v[48:63], v[96:99], v[88:91], v[48:63]\n\t" \
    "ds_read_b64_tr_b16 v[96:97], %[wb] offset:57344\n\t" \
    "ds_read_b64_tr_b16 v[98:99], %[wb] offset:58368\n\t" \
    "ds_read_b128 v[88:91], %[x2] offset:49152\n\t" \
    "v_mfma_f32_32x32x16_bf16 v[32:47], v[104:107], v[100:103], v[32:47]\n\t" \
    "ds_read_b64_tr_b16 v[104:105], %[wa] offset:61440\n\t" \
    "ds_read_b64_tr_b16 v[106:107], %[wa] offset:62464\n\t" \
    "v_mfma_f32_32x32x16_bf16 v[48:63], v[108:111], v[100:103], v[48:63]\n\t" \
    "ds_read_b64_tr_b16 v[108:109], %[wb] offset:61440\n\t" \
    "ds_read_b64_tr_b16 v[110:111], %[wb] offset:62464\n\t" \
    "ds_read_b128 v[100:103], %[x3] offset:49152\n\t" \
    "s_sub_u32 %[n], %[n], 1\n\t" \
    "s_cmp_eq_u32 %[n], 0\n\t" \
    "s_cbranch_scc1 2f\n\t" \
    "s_waitcnt lgkmcnt(15)\n\t" \
    "v_mfma_f32_32x32x16_bf16 v[32:47], v[68:71], v[64:67], v[32:47]\n\t" \
    "v_mfma_f32_32x32x16_bf16 v[48:63], v[72:75], v[64:67], v[48:63]\n\t" \
    "s_waitcnt lgkmcnt(0)\n\t" \
    "s_barrier\n\t" \
    "ds_read_b64_tr_b16 v[68:69], %[wa] offset:0\n\t" \
    "ds_read_b64_tr_b16 v[70:71], %[wa] offset:1024\n\t" \
    "ds_read_b64_tr_b16 v[72:73], %[wb] offset:0\n\t" \
    "ds_read_b64_tr_b16 v[74:75], %[wb] offset:1024\n\t" \
    "ds_read_b128 v[64:67], %[x0] offset:0\n\t" \
    "v_mfma_f32_32x32x16_bf16 v[32:47], v[80:83], v[76:79], v[32:47]\n\t" \
    "ds_read_b64_tr_b16 v[80:81], %[wa] offset:4096\n\t" \
    "ds_read_b64_tr_b16 v[82:83], %[wa] offset:5120\n\t" \
    "v_mfma_f32_32x32x16_bf16 v[48:63], v[84:87], v[76:79], v[48:63]\n\t" \
    "ds_read_b64_tr_b16 v[84:85], %[wb] offset:4096\n\t" \
    "ds_read_b64_tr_b16 v[86:87], %[wb] offset:5120\n\t" \
    "ds_read_b128 v[76:79], %[x1] offset:0\n\t" \
    "v_mfma_f32_32x32x16_bf16 v[32:47], v[92:95], v[88:91], v[32:47]\n\t" \
    "ds_read_b64_tr_b16 v[92:93], %[wa] offset:8192\n\t" \
    "ds_read_b64_tr_b16 v[94:95], %[wa] offset:9216\n\t" \
    "v_mfma_f32_32x32x16_bf16 v[48:63], v[96:99], v[88:91], v[48:63]\n\t" \
    "ds_read_b64_tr_b16 v[96:97], %[wb] offset:8192\n\t" \
    "ds_read_b64_tr_b16 v[98:99], %[wb] offset:9216\n\t" \
    "ds_read_b128 v[88:91], %[x2] offset:0\n\t" \
    "v_mfma_f32_32x32x16_bf16 v[32:47], v[104:107], v[100:103], v[32:47]\n\t" \
    "ds_read_b64_tr_b16 v[104:105], %[wa] offset:12288\n\t" \
    "ds_read_b64_tr_b16 v[106:107], %[wa] offset:13312\n\t" \
    "v_mfma_f32_32x32x16_bf16 v[48:63], v[108:111], v[100:103], v[48:63]\n\t" \
    "ds_read_b64_tr_b16 v[108:109], %[wb] offset:12288\n\t" \
    "ds_read_b64_tr_b16 v[110:111], %[wb] offset:13312\n\t" \
    "ds_read_b128 v[100:103], %[x3] offset:0\n\t" \
    "s_sub_u32 %[n], %[n], 1\n\t" \
    "s_cmp_eq_u32 %[n], 0\n\t" \
    "s_cbranch_scc1 2f\n\t" \
    "s_waitcnt lgkmcnt(15)\n\t" \
    "v_mfma_f32_32x32x16_bf16 v[32:47], v[68:71], v[64:67], v[32:47]\n\t" \
    "v_mfma_f32_32x32x16_bf16 v[48:63], v[72:75], v[64:67], v[48:63]\n\t" \
    "s_waitcnt vmcnt(0) lgkmcnt(0)\n\t" \
    "s_barrier\n\t" \
    "ds_read_b64_tr_b16 v[68:69], %[wa] offset:24576\n\t" \
    "ds_read_b64_tr_b16 v[70:71], %[wa] offset:25600\n\t" \
    "ds_read_b64_tr_b16 v[72:73], %[wb] offset:24576\n\t" \
    "ds_read_b64_tr_b16 v[74:75], %[wb] offset:25600\n\t" \
    "ds_read_b128 v[64:67], %[x0] offset:24576\n\t" \
    "v_mfma_f32_32x32x16_bf16 v[32:47], v[80:83], v[76:79], v[32:47]\n\t" \
    "ds_read_b64_tr_b16 v[80:81], %[wa] offset:28672\n\t" \
    "ds_read_b64_tr_b16 v[82:83], %[wa] offset:29696\n\t" \
    "v_mfma_f32_32x32x16_bf16 v[48:63], v[84:87], v[76:79], v[48:63]\n\t" \
    "ds_read_b64_tr_b16 v[84:85], %[wb] offset:28672\n\t" \
    "ds_read_b64_tr_b16 v[86:87], %[wb] offset:29696\n\t" \
    "ds_read_b128 v[76:79], %[x1] offset:24576\n\t" \
    "v_mfma_f32_32x32x16_bf16 v[32:47], v[92:95], v[88:91], v[32:47]\n\t" \
    "ds_read_b64_tr_b16 v[92:93], %[wa] offset:32768\n\t" \
    "ds_read_b64_tr_b16 v[94:95], %[wa] offset:33792\n\t" \
    "v_mfma_f32_32x32x16_bf16 v[48:63], v[96:99], v[88:91], v[48:63]\n\t" \
    "ds_read_b64_tr_b16 v[96:97], %[wb] offset:32768\n\t" \
    "ds_read_b64_tr_b16 v[98:99], %[wb] offset:33792\n\t" \
    "ds_read_b128 v[88:91], %[x2] offset:24576\n\t" \
    "v_mfma_f32_32x32x16_bf16 v[32:47], v[104:107], v[100:103], v[32:47]\n\t" \
    "ds_read_b64_tr_b16 v[104:105], %[wa] offset:36864\n\t" \
    "ds_read_b64_tr_b16 v[106:107], %[wa] offset:37888\n\t" \
    "v_mfma_f32_32x32x16_bf16 v[48:63], v[108:111], v[100:103], v[48:63]\n\t" \
    "ds_read_b64_tr_b16 v[108:109], %[wb] offset:36864\n\t" \
    "ds_read_b64_tr_b16 v[110:111], %[wb] offset:37888\n\t" \
    "ds_read_b128 v[100:103], %[x3] offset:24576\n\t" \
    "s_sub_u32 %[n], %[n], 1\n\t" \
    "s_cmp_eq_u32 %[n], 0\n\t" \
    "s_cbranch_scc0 1b\n\t" \
    "2:\n\t" \
    "s_waitcnt lgkmcnt(15)\n\t" \
    "v_mfma_f32_32x32x16_bf16 v[32:47], v[68:71], v[64:67], v[32:47]\n\t" \
    "v_mfma_f32_32x32x16_bf16 v[48:63], v[72:75], v[64:67], v[48:63]\n\t" \
    "s_waitcnt lgkmcnt(10)\n\t" \
    "v_mfma_f32_32x32x16_bf16 v[32:47], v[80:83], v[76:79], v[32:47]\n\t" \
    "v_mfma_f32_32x32x16_bf16 v[48:63], v[84:87], v[76:79], v[48:63]\n\t" \
    "s_waitcnt lgkmcnt(5)\n\t" \
    "v_mfma_f32_32x32x16_bf16 v[32:47], v[92:95], v[88:91], v[32:47]\n\t" \
    "v_mfma_f32_32x32x16_bf16 v[48:63], v[96:99], v[88:91], v[48:63]\n\t" \
    "s_waitcnt lgkmcnt(0)\n\t" \
    "v_mfma_f32_32x32x16_bf16 v[32:47], v[104:107], v[100:103], v[32:47]\n\t" \
    "v_mfma_f32_32x32x16_bf16 v[48:63], v[108:111], v[100:103], v[48:63]\n\t" \
    "3:\n\t" \
    "s_waitcnt lgkmcnt(0)\n\t" \
    "s_nop 11\n\t"
#define ASM_E1 \
    "s_barrier\n\t" \
    "ds_read_b64_tr_b16 v[68:69], %[wa] offset:0\n\t" \
    "ds_read_b64_tr_b16 v[70:71], %[wa] offset:1024\n\t" \
    "ds_read_b64_tr_b16 v[72:73], %[wb] offset:0\n\t" \
    "ds_read_b64_tr_b16 v[74:75], %[wb] offset:1024\n\t" \
    "ds_read_b128 v[64:67], %[x0] offset:0\n\t" \
    "ds_read_b64_tr_b16 v[80:81], %[wa] offset:4096\n\t" \
    "ds_read_b64_tr_b16 v[82:83], %[wa] offset:5120\n\t" \
    "ds_read_b64_tr_b16 v[84:85], %[wb] offset:4096\n\t" \
    "ds_read_b64_tr_b16 v[86:87], %[wb] offset:5120\n\t" \
    "ds_read_b128 v[76:79], %[x1] offset:0\n\t" \
    "ds_read_b64_tr_b16 v[92:93], %[wa] offset:8192\n\t" \
    "ds_read_b64_tr_b16 v[94:95], %[wa] offset:9216\n\t" \
    "ds_read_b64_tr_b16 v[96:97], %[wb] offset:8192\n\t" \
    "ds_read_b64_tr_b16 v[98:99], %[wb] offset:9216\n\t" \
    "ds_read_b128 v[88:91], %[x2] offset:0\n\t" \
    "ds_read_b64_tr_b16 v[104:105], %[wa] offset:12288\n\t" \
    "ds_read_b64_tr_b16 v[106:107], %[wa] offset:13312\n\t" \
    "ds_read_b64_tr_b16 v[108:109], %[wb] offset:12288\n\t" \
    "ds_read_b64_tr_b16 v[110:111], %[wb] offset:13312\n\t" \
    "ds_read_b128 v[100:103], %[x3] offset:0\n\t" \
    "1:\n\t" \
    "s_waitcnt lgkmcnt(15)\n\t" \
    "v_mfma_f32_32x32x16_bf16 v[32:47], v[68:71], v[64:67], v[32:47]\n\t" \
    "v_mfma_f32_32x32x16_bf16 v[48:63], v[72:75], v[64:67], v[48:63]\n\t" \
    "s_waitcnt lgkmcnt(0)\n\t" \
    "s_barrier\n\t" \
    "ds_read_b64_tr_b16 v[68:69], %[wa] offset:24576\n\t" \
    "ds_read_b64_tr_b16 v[70:71], %[wa] offset:25600\n\t" \
    "ds_read_b64_tr_b16 v[72:73], %[wb] offset:24576\n\t" \
    "ds_read_b64_tr_b16 v[74:75], %[wb] offset:25600\n\t" \
    "ds_read_b128 v[64:67], %[x0] offset:24576\n\t" \
    "v_mfma_f32_32x32x16_bf16 v[32:47], v[80:83], v[76:79], v[32:47]\n\t" \
    "ds_read_b64_tr_b16 v[80:81], %[wa] offset:28672\n\t" \
    "ds_read_b64_tr_b16 v[82:83], %[wa] offset:29696\n\t" \
    "v_mfma_f32_32x32x16_bf16 v[48:63], v[84:87], v[76:79], v[48:63]\n\t" \
    "ds_read_b64_tr_b16 v[84:85], %[wb] offset:28672\n\t" \
    "ds_read_b64_tr_b16 v[86:87], %[wb] offset:29696\n\t" \
    "ds_read_b128 v[76:79], %[x1] offset:24576\n\t" \
    "v_mfma_f32_32x32x16_bf16 v[32:47], v[92:95], v[88:91], v[32:47]\n\t" \
    "ds_read_b64_tr_b16 v[92:93], %[wa] offset:32768\n\t" \
    "ds_read_b64_tr_b16 v[94:95], %[wa] offset:33792\n\t" \
    "v_mfma_f32_32x32x16_bf16 v[48:63], v[96:99], v[88:91], v[48:63]\n\t" \
    "ds_read_b64_tr_b16 v[96:97], %[wb] offset:32768\n\t" \
    "ds_read_b64_tr_b16 v[98:99], %[wb] offset:33792\n\t" \
    "ds_read_b128 v[88:91], %[x2] offset:24576\n\t" \
    "v_mfma_f32_32x32x16_bf16 v[32:47], v[104:107], v[100:103], v[32:47]\n\t" \
    "ds_read_b64_tr_b16 v[104:105], %[wa] offset:36864\n\t" \
    "ds_read_b64_tr_b16 v[106:107], %[wa] offset:37888\n\t" \
    "v_mfma_f32_32x32x16_bf16 v[48:63], v[108:111], v[100:103], v[48:63]\n\t" \
    "ds_read_b64_tr_b16 v[108:109], %[wb] offset:36864\n\t" \
    "ds_read_b64_tr_b16 v[110:111], %[wb] offset:37888\n\t" \
    "ds_read_b128 v[100:103], %[x3] offset:24576\n\t" \
    "s_sub_u32 %[n], %[n], 1\n\t" \
    "s_cmp_eq_u32 %[n], 0\n\t" \
    "s_cbranch_scc1 2f\n\t" \
    "s_waitcnt lgkmcnt(15)\n\t" \
    "v_mfma_f32_32x32x16_bf16 v[32:47], v[68:71], v[64:67], v[32:47]\n\t" \
    "v_mfma_f32_32x32x16_bf16 v[48:63], v[72:75], v[64:67], v[48:63]\n\t" \
    "s_waitcnt lgkmcnt(0)\n\t" \
    "s_barrier\n\t" \
    "ds_read_b64_tr_b16 v[68:69], %[wa] offset:49152\n\t" \
    "ds_read_b64_tr_b16 v[70:71], %[wa] offset:50176\n\t" \
    "ds_read_b64_tr_b16 v[72:73], %[wb] offset:49152\n\t" \
    "ds_read_b64_tr_b16 v[74:75], %[wb] offset:50176\n\t" \
    "ds_read_b128 v[64:67], %[x0] offset:49152\n\t" \
    "v_mfma_f32_32x32x16_bf16 v[32:47], v[80:83], v[76:79], v[32:47]\n\t" \
    "ds_read_b64_tr_b16 v[80:81], %[wa] offset:53248\n\t" \
    "ds_read_b64_tr_b16 v[82:83], %[wa] offset:54272\n\t" \
    "v_mfma_f32_32x32x16_bf16 v[48:63], v[84:87], v[76:79], v[48:63]\n\t" \
    "ds_read_b64_tr_b16 v[84:85], %[wb] offset:53248\n\t" \
    "ds_read_b64_tr_b16 v[86:87], %[wb] offset:54272\n\t" \
    "ds_read_b128 v[76:79], %[x1] offset:49152\n\t" \
    "v_mfma_f32_32x32x16_bf16 v[32:47], v[92:95], v[88:91], v[32:47]\n\t" \
    "ds_read_b64_tr_b16 v[92:93], %[wa] offset:57344\n\t" \
    "ds_read_b64_tr_b16 v[94:95], %[wa] offset:58368\n\t" \
    "v_mfma_f32_32x32x16_bf16 v[48:63], v[96:99], v[88:91], v[48:63]\n\t" \
    "ds_read_b64_tr_b16 v[96:97], %[wb] offset:57344\n\t" \
    "ds_read_b64_tr_b16 v[98:99], %[wb] offset:58368\n\t" \
    "ds_read_b128 v[88:91], %[x2] offset:49152\n\t" \
    "v_mfma_f32_32x32x16_bf16 v[32:47], v[104:107], v[100:103], v[32:47]\n\t" \
    "ds_read_b64_tr_b16 v[104:105], %[wa] offset:61440\n\t" \
    "ds_read_b64_tr_b16 v[106:107], %[wa] offset:62464\n\t" \
    "v_mfma_f32_32x32x16_bf16 v[48:63], v[108:111], v[100:103], v[48:63]\n\t" \
    "ds_read_b64_tr_b16 v[108:109], %[wb] offset:61440\n\t" \
    "ds_read_b64_tr_b16 v[110:111], %[wb] offset:62464\n\t" \
    "ds_read_b128 v[100:103], %[x3] offset:49152\n\t" \
    "s_sub_u32 %[n], %[n], 1\n\t" \
    "s_cmp_eq_u32 %[n], 0\n\t" \
    "s_cbranch_scc1 2f\n\t" \
    "s_waitcnt lgkmcnt(15)\n\t" \
    "v_mfma_f32_32x32x16_bf16 v[32:47], v[68:71], v[64:67], v[32:47]\n\t" \
    "v_mfma_f32_32x32x16_bf16 v[48:63], v[72:75], v[64:67], v[48:63]\n\t" \
    "s_waitcnt vmcnt(0) lgkmcnt(0)\n\t" \
    "s_barrier\n\t" \
    "ds_read_b64_tr_b16 v[68:69], %[wa] offset:0\n\t" \
    "ds_read_b64_tr_b16 v[70:71], %[wa] offset:1024\n\t" \
    "ds_read_b64_tr_b16 v[72:73], %[wb] offset:0\n\t" \
    "ds_read_b64_tr_b16 v[74:75], %[wb] offset:1024\n\t" \
    "ds_read_b128 v[64:67], %[x0] offset:0\n\t" \
    "v_mfma_f32_32x32x16_bf16 v[32:47], v[80:83], v[76:79], v[32:47]\n\t" \
    "ds_read_b64_tr_b16 v[80:81], %[wa] offset:4096\n\t" \
    "ds_read_b64_tr_b16 v[82:83], %[wa] offset:5120\n\t" \
    "v_mfma_f32_32x32x16_bf16 v[48:63], v[84:87], v[76:79], v[48:63]\n\t" \
    "ds_read_b64_tr_b16 v[84:85], %[wb] offset:4096\n\t" \
    "ds_read_b64_tr_b16 v[86:87], %[wb] offset:5120\n\t" \
    "ds_read_b128 v[76:79], %[x1] offset:0\n\t" \
    "v_mfma_f32_32x32x16_bf16 v[32:47], v[92:95], v[88:91], v[32:47]\n\t" \
    "ds_read_b64_tr_b16 v[92:93], %[wa] offset:8192\n\t" \
    "ds_read_b64_tr_b16 v[94:95], %[wa] offset:9216\n\t" \
    "v_mfma_f32_32x32x16_bf16 v[48:63], v[96:99], v[88:91], v[48:63]\n\t" \
    "ds_read_b64_tr_b16 v[96:97], %[wb] offset:8192\n\t" \
    "ds_read_b64_tr_b16 v[98:99], %[wb] offset:9216\n\t" \
    "ds_read_b128 v[88:91], %[x2] offset:0\n\t" \
    "v_mfma_f32_32x32x16_bf16 v[32:47], v[104:107], v[100:103], v[32:47]\n\t" \
    "ds_read_b64_tr_b16 v[104:105], %[wa] offset:12288\n\t" \
    "ds_read_b64_tr_b16 v[106:107], %[wa] offset:13312\n\t" \
    "v_mfma_f32_32x32x16_bf16 v[48:63], v[108:111], v[100:103], v[48:63]\n\t" \
    "ds_read_b64_tr_b16 v[108:109], %[wb] offset:12288\n\t" \
    "ds_read_b64_tr_b16 v[110:111], %[wb] offset:13312\n\t" \
    "ds_read_b128 v[100:103], %[x3] offset:0\n\t" \
    "s_sub_u32 %[n], %[n], 1\n\t" \
    "s_cmp_eq_u32 %[n], 0\n\t" \
    "s_cbranch_scc0 1b\n\t" \
    "2:\n\t" \
    "s_waitcnt lgkmcnt(15)\n\t" \
    "v_mfma_f32_32x32x16_bf16 v[32:47], v[68:71], v[64:67], v[32:47]\n\t" \
    "v_mfma_f32_32x32x16_bf16 v[48:63], v[72:75], v[64:67], v[48:63]\n\t" \
    "s_waitcnt lgkmcnt(10)\n\t" \
    "v_mfma_f32_32x32x16_bf16 v[32:47], v[80:83], v[76:79], v[32:47]\n\t" \
    "v_mfma_f32_32x32x16_bf16 v[48:63], v[84:87], v[76:79], v[48:63]\n\t" \
    "s_waitcnt lgkmcnt(5)\n\t" \
    "v_mfma_f32_32x32x16_bf16 v[32:47], v[92:95], v[88:91], v[32:47]\n\t" \
    "v_mfma_f32_32x32x16_bf16 v[48:63], v[96:99], v[88:91], v[48:63]\n\t" \
    "s_waitcnt lgkmcnt(0)\n\t" \
    "v_mfma_f32_32x32x16_bf16 v[32:47], v[104:107], v[100:103], v[32:47]\n\t" \
    "v_mfma_f32_32x32x16_bf16 v[48:63], v[108:111], v[100:103], v[48:63]\n\t" \
    "3:\n\t" \
    "s_waitcnt lgkmcnt(0)\n\t" \
    "s_nop 11\n\t"
#define ASM_96T \
    "s_barrier\n\t" \
    "ds_read_b64_tr_b16 v[68:69], %[wa] offset:0\n\t" \
    "ds_read_b64_tr_b16 v[70:71], %[wa] offset:768\n\t" \
    "ds_read_b64_tr_b16 v[72:73], %[wa] offset:64\n\t" \
    "ds_read_b64_tr_b16 v[74:75], %[wa] offset:832\n\t" \
    "ds_read_b128 v[64:67], %[x0] offset:0\n\t" \
    "ds_read_b64_tr_b16 v[80:81], %[wa] offset:3072\n\t" \
    "ds_read_b64_tr_b16 v[82:83], %[wa] offset:3840\n\t" \
    "ds_read_b64_tr_b16 v[84:85], %[wa] offset:3136\n\t" \
    "ds_read_b64_tr_b16 v[86:87], %[wa] offset:3904\n\t" \
    "ds_read_b128 v[76:79], %[x1] offset:0\n\t" \
    "ds_read_b64_tr_b16 v[92:93], %[wa] offset:6144\n\t" \
    "ds_read_b64_tr_b16 v[94:95], %[wa] offset:6912\n\t" \
    "ds_read_b64_tr_b16 v[96:97], %[wa] offset:6208\n\t" \
    "ds_read_b64_tr_b16 v[98:99], %[wa] offset:6976\n\t" \
    "ds_read_b128 v[88:91], %[x2] offset:0\n\t" \
    "ds_read_b64_tr_b16 v[104:105], %[wa] offset:9216\n\t" \
    "ds_read_b64_tr_b16 v[106:107], %[wa] offset:9984\n\t" \
    "ds_read_b64_tr_b16 v[108:109], %[wa] offset:9280\n\t" \
    "ds_read_b64_tr_b16 v[110:111], %[wa] offset:10048\n\t" \
    "ds_read_b128 v[100:103], %[x3] offset:0\n\t" \
    "1:\n\t" \
    "s_waitcnt lgkmcnt(15)\n\t" \
    "v_mfma_f32_32x32x16_bf16 v[32:47], v[68:71], v[64:67], v[32:47]\n\t" \
    "v_mfma_f32_32x32x16_bf16 v[48:63], v[72:75], v[64:67], v[48:63]\n\t" \
    "s_waitcnt lgkmcnt(0)\n\t" \
    "s_barrier\n\t" \
    "ds_read_b64_tr_b16 v[68:69], %[wa] offset:20480\n\t" \
    "ds_read_b64_tr_b16 v[70:71], %[wa] offset:21248\n\t" \
    "ds_read_b64_tr_b16 v[72:73], %[wa] offset:20544\n\t" \
    "ds_read_b64_tr_b16 v[74:75], %[wa] offset:21312\n\t" \
    "ds_read_b128 v[64:67], %[x0] offset:20480\n\t" \
    "v_mfma_f32_32x32x16_bf16 v[32:47], v[80:83], v[76:79], v[32:47]\n\t" \
    "ds_read_b64_tr_b16 v[80:81], %[wa] offset:23552\n\t" \
    "ds_read_b64_tr_b16 v[82:83], %[wa] offset:24320\n\t" \
    "v_mfma_f32_32x32x16_bf16 v[48:63], v[84:87], v[76:79], v[48:63]\n\t" \
    "ds_read_b64_tr_b16 v[84:85], %[wa] offset:23616\n\t" \
    "ds_read_b64_tr_b16 v[86:87], %[wa] offset:24384\n\t" \
    "ds_read_b128 v[76:79], %[x1] offset:20480\n\t" \
    "v_mfma_f32_32x32x16_bf16 v[32:47], v[92:95], v[88:91], v[32:47]\n\t" \
    "ds_read_b64_tr_b16 v[92:93], %[wa] offset:26624\n\t" \
    "ds_read_b64_tr_b16 v[94:95], %[wa] offset:27392\n\t" \
    "v_mfma_f32_32x32x16_bf16 v[48:63], v[96:99], v[88:91], v[48:63]\n\t" \
    "ds_read_b64_tr_b16 v[96:97], %[wa] offset:26688\n\t" \
    "ds_read_b64_tr_b16 v[98:99], %[wa] offset:27456\n\t" \
    "ds_read_b128 v[88:91], %[x2] offset:20480\n\t" \
    "v_mfma_f32_32x32x16_bf16 v[32:47], v[104:107], v[100:103], v[32:47]\n\t" \
    "ds_read_b64_tr_b16 v[104:105], %[wa] offset:29696\n\t" \
    "ds_read_b64_tr_b16 v[106:107], %[wa] offset:30464\n\t" \
    "v_mfma_f32_32x32x16_bf16 v[48:63], v[108:111], v[100:103], v[48:63]\n\t" \
    "ds_read_b64_tr_b16 v[108:109], %[wa] offset:29760\n\t" \
    "ds_read_b64_tr_b16 v[110:111], %[wa] offset:30528\n\t" \
    "ds_read_b128 v[100:103], %[x3] offset:20480\n\t" \
    "s_sub_u32 %[n], %[n], 1\n\t" \
    "s_cmp_eq_u32 %[n], 0\n\t" \
    "s_cbranch_scc1 2f\n\t" \
    "s_waitcnt lgkmcnt(15)\n\t" \
    "v_mfma_f32_32x32x16_bf16 v[32:47], v[68:71], v[64:67], v[32:47]\n\t" \
    "v_mfma_f32_32x32x16_bf16 v[48:63], v[72:75], v[64:67], v[48:63]\n\t" \
    "s_waitcnt lgkmcnt(0)\n\t" \
    "s_barrier\n\t" \
    "ds_read_b64_tr_b16 v[68:69], %[wa] offset:40960\n\t" \
    "ds_read_b64_tr_b16 v[70:71], %[wa] offset:41728\n\t" \
    "ds_read_b64_tr_b16 v[72:73], %[wa] offset:41024\n\t" \
    "ds_read_b64_tr_b16 v[74:75], %[wa] offset:41792\n\t" \
    "ds_read_b128 v[64:67], %[x0] offset:40960\n\t" \
    "v_mfma_f32_32x32x16_bf16 v[32:47], v[80:83], v[76:79], v[32:47]\n\t" \
    "ds_read_b64_tr_b16 v[80:81], %[wa] offset:44032\n\t" \
    "ds_read_b64_tr_b16 v[82:83], %[wa] offset:44800\n\t" \
    "v_mfma_f32_32x32x16_bf16 v[48:63], v[84:87], v[76:79], v[48:63]\n\t" \
    "ds_read_b64_tr_b16 v[84:85], %[wa] offset:44096\n\t" \
    "ds_read_b64_tr_b16 v[86:87], %[wa] offset:44864\n\t" \
    "ds_read_b128 v[76:79], %[x1] offset:40960\n\t" \
    "v_mfma_f32_32x32x16_bf16 v[32:47], v[92:95], v[88:91], v[32:47]\n\t" \
    "ds_read_b64_tr_b16 v[92:93], %[wa] offset:47104\n\t" \
    "ds_read_b64_tr_b16 v[94:95], %[wa] offset:47872\n\t" \
    "v_mfma_f32_32x32x16_bf16 v[48:63], v[96:99], v[88:91], v[48:63]\n\t" \
    "ds_read_b64_tr_b16 v[96:97], %[wa] offset:47168\n\t" \
    "ds_read_b64_tr_b16 v[98:99], %[wa] offset:47936\n\t" \
    "ds_read_b128 v[88:91], %[x2] offset:40960\n\t" \
    "v_mfma_f32_32x32x16_bf16 v[32:47], v[104:107], v[100:103], v[32:47]\n\t" \
    "ds_read_b64_tr_b16 v[104:105], %[wa] offset:50176\n\t" \
    "ds_read_b64_tr_b16 v[106:107], %[wa] offset:50944\n\t" \
    "v_mfma_f32_32x32x16_bf16 v[48:63], v[108:111], v[100:103], v[48:63]\n\t" \
    "ds_read_b64_tr_b16 v[108:109], %[wa] offset:50240\n\t" \
    "ds_read_b64_tr_b16 v[110:111], %[wa] offset:51008\n\t" \
    "ds_read_b128 v[100:103], %[x3] offset:40960\n\t" \
    "s_sub_u32 %[n], %[n], 1\n\t" \
    "s_cmp_eq_u32 %[n], 0\n\t" \
    "s_cbranch_scc1 2f\n\t" \
    "s_waitcnt lgkmcnt(15)\n\t" \
    "v_mfma_f32_32x32x16_bf16 v[32:47], v[68:71], v[64:67], v[32:47]\n\t" \
    "v_mfma_f32_32x32x16_bf16 v[48:63], v[72:75], v[64:67], v[48:63]\n\t" \
    "s_waitcnt lgkmcnt(0)\n\t" \
    "s_barrier\n\t" \
    "ds_read_b64_tr_b16 v[68:69], %[wa] offset:0\n\t" \
    "ds_read_b64_tr_b16 v[70:71], %[wa] offset:768\n\t" \
    "ds_read_b64_tr_b16 v[72:73], %[wa] offset:64\n\t" \
    "ds_read_b64_tr_b16 v[74:75], %[wa] offset:832\n\t" \
    "ds_read_b128 v[64:67], %[x0] offset:0\n\t" \
    "v_mfma_f32_32x32x16_bf16 v[32:47], v[80:83], v[76:79], v[32:47]\n\t" \
    "ds_read_b64_tr_b16 v[80:81], %[wa] offset:3072\n\t" \
    "ds_read_b64_tr_b16 v[82:83], %[wa] offset:3840\n\t" \
    "v_mfma_f32_32x32x16_bf16 v[48:63], v[84:87], v[76:79], v[48:63]\n\t" \
    "ds_read_b64_tr_b16 v[84:85], %[wa] offset:3136\n\t" \
    "ds_read_b64_tr_b16 v[86:87], %[wa] offset:3904\n\t" \
    "ds_read_b128 v[76:79], %[x1] offset:0\n\t" \
    "v_mfma_f32_32x32x16_bf16 v[32:47], v[92:95], v[88:91], v[32:47]\n\t" \
    "ds_read_b64_tr_b16 v[92:93], %[wa] offset:6144\n\t" \
    "ds_read_b64_tr_b16 v[94:95], %[wa] offset:6912\n\t" \
    "v_mfma_f32_32x32x16_bf16 v[48:63], v[96:99], v[88:91], v[48:63]\n\t" \
    "ds_read_b64_tr_b16 v[96:97], %[wa] offset:6208\n\t" \
    "ds_read_b64_tr_b16 v[98:99], %[wa] offset:6976\n\t" \
    "ds_read_b128 v[88:91], %[x2] offset:0\n\t" \
    "v_mfma_f32_32x32x16_bf16 v[32:47], v[104:107], v[100:103], v[32:47]\n\t" \
    "ds_read_b64_tr_b16 v[104:105], %[wa] offset:9216\n\t" \
    "ds_read_b64_tr_b16 v[106:107], %[wa] offset:9984\n\t" \
    "v_mfma_f32_32x32x16_bf16 v[48:63], v[108:111], v[100:103], v[48:63]\n\t" \
    "ds_read_b64_tr_b16 v[108:109], %[wa] offset:9280\n\t" \
    "ds_read_b64_tr_b16 v[110:111], %[wa] offset:10048\n\t" \
    "ds_read_b128 v[100:103], %[x3] offset:0\n\t" \
    "s_sub_u32 %[n], %[n], 1\n\t" \
    "s_cmp_eq_u32 %[n], 0\n\t" \
    "s_cbranch_scc0 1b\n\t" \
    "2:\n\t" \
    "s_waitcnt lgkmcnt(15)\n\t" \
    "v_mfma_f32_32x32x16_bf16 v[32:47], v[68:71], v[64:67], v[32:47]\n\t" \
    "v_mfma_f32_32x32x16_bf16 v[48:63], v[72:75], v[64:67], v[48:63]\n\t" \
    "s_waitcnt lgkmcnt(10)\n\t" \
    "v_mfma_f32_32x32x16_bf16 v[32:47], v[80:83], v[76:79], v[32:47]\n\t" \
    "v_mfma_f32_32x32x16_bf16 v[48:63], v[84:87], v[76:79], v[48:63]\n\t" \
    "s_cmp_eq_u32 %[kl], 2\n\t" \
    "s_cbranch_scc1 3f\n\t" \
    "s_waitcnt lgkmcnt(5)\n\t" \
    "v_mfma_f32_32x32x16_bf16 v[32:47], v[92:95], v[88:91], v[32:47]\n\t" \
    "v_mfma_f32_32x32x16_bf16 v[48:63], v[96:99], v[88:91], v[48:63]\n\t" \
    "s_waitcnt lgkmcnt(0)\n\t" \
    "v_mfma_f32_32x32x16_bf16 v[32:47], v[104:107], v[100:103], v[32:47]\n\t" \
    "v_mfma_f32_32x32x16_bf16 v[48:63], v[108:111], v[100:103], v[48:63]\n\t" \
    "3:\n\t" \
    "s_waitcnt lgkmcnt(0)\n\t" \
    "s_nop 11\n\t"
#define ASM_96S \
    "s_barrier\n\t" \
    "ds_read_b64_tr_b16 v[68:69], %[wa] offset:0\n\t" \
    "ds_read_b64_tr_b16 v[70:71], %[wa] offset:768\n\t" \
    "ds_read_b128 v[64:67], %[x0] offset:0\n\t" \
    "ds_read_b64_tr_b16 v[80:81], %[wa] offset:3072\n\t" \
    "ds_read_b64_tr_b16 v[82:83], %[wa] offset:3840\n\t" \
    "ds_read_b128 v[76:79], %[x1] offset:0\n\t" \
    "ds_read_b64_tr_b16 v[92:93], %[wa] offset:6144\n\t" \
    "ds_read_b64_tr_b16 v[94:95], %[wa] offset:6912\n\t" \
    "ds_read_b128 v[88:91], %[x2] offset:0\n\t" \
    "ds_read_b64_tr_b16 v[104:105], %[wa] offset:9216\n\t" \
    "ds_read_b64_tr_b16 v[106:107], %[wa] offset:9984\n\t" \
    "ds_read_b128 v[100:103], %[x3] offset:0\n\t" \
    "1:\n\t" \
    "s_waitcnt lgkmcnt(9)\n\t" \
    "v_mfma_f32_32x32x16_bf16 v[32:47], v[68:71], v[64:67], v[32:47]\n\t" \
    "s_waitcnt lgkmcnt(0)\n\t" \
    "s_barrier\n\t" \
    "ds_read_b64_tr_b16 v[68:69], %[wa] offset:20480\n\t" \
    "ds_read_b64_tr_b16 v[70:71], %[wa] offset:21248\n\t" \
    "ds_read_b128 v[64:67], %[x0] offset:20480\n\t" \
    "v_mfma_f32_32x32x16_bf16 v[32:47], v[80:83], v[76:79], v[32:47]\n\t" \
    "ds_read_b64_tr_b16 v[80:81], %[wa] offset:23552\n\t" \
    "ds_read_b64_tr_b16 v[82:83], %[wa] offset:24320\n\t" \
    "ds_read_b128 v[76:79], %[x1] offset:20480\n\t" \
    "v_mfma_f32_32x32x16_bf16 v[32:47], v[92:95], v[88:91], v[32:47]\n\t" \
    "ds_read_b64_tr_b16 v[92:93], %[wa] offset:26624\n\t" \
    "ds_read_b64_tr_b16 v[94:95], %[wa] offset:27392\n\t" \
    "ds_read_b128 v[88:91], %[x2] offset:20480\n\t" \
    "v_mfma_f32_32x32x16_bf16 v[32:47], v[104:107], v[100:103], v[32:47]\n\t" \
    "ds_read_b64_tr_b16 v[104:105], %[wa] offset:29696\n\t" \
    "ds_read_b64_tr_b16 v[106:107], %[wa] offset:30464\n\t" \
    "ds_read_b128 v[100:103], %[x3] offset:20480\n\t" \
    "s_sub_u32 %[n], %[n], 1\n\t" \
    "s_cmp_eq_u32 %[n], 0\n\t" \
    "s_cbranch_scc1 2f\n\t" \
    "s_waitcnt lgkmcnt(9)\n\t" \
    "v_mfma_f32_32x32x16_bf16 v[32:47], v[68:71], v[64:67], v[32:47]\n\t" \
    "s_waitcnt lgkmcnt(0)\n\t" \
    "s_barrier\n\t" \
    "ds_read_b64_tr_b16 v[68:69], %[wa] offset:40960\n\t" \
    "ds_read_b64_tr_b16 v[70:71], %[wa] offset:41728\n\t" \
    "ds_read_b128 v[64:67], %[x0] offset:40960\n\t" \
    "v_mfma_f32_32x32x16_bf16 v[32:47], v[80:83], v[76:79], v[32:47]\n\t" \
    "ds_read_b64_tr_b16 v[80:81], %[wa] offset:44032\n\t" \
    "ds_read_b64_tr_b16 v[82:83], %[wa] offset:44800\n\t" \
    "ds_read_b128 v[76:79], %[x1] offset:40960\n\t" \
    "v_mfma_f32_32x32x16_bf16 v[32:47], v[92:95], v[88:91], v[32:47]\n\t" \
    "ds_read_b64_tr_b16 v[92:93], %[wa] offset:47104\n\t" \
    "ds_read_b64_tr_b16 v[94:95], %[wa] offset:47872\n\t" \
    "ds_read_b128 v[88:91], %[x2] offset:40960\n\t" \
    "v_mfma_f32_32x32x16_bf16 v[32:47], v[104:107], v[100:103], v[32:47]\n\t" \
    "ds_read_b64_tr_b16 v[104:105], %[wa] offset:50176\n\t" \
    "ds_read_b64_tr_b16 v[106:107], %[wa] offset:50944\n\t" \
    "ds_read_b128 v[100:103], %[x3] offset:40960\n\t" \
    "s_sub_u32 %[n], %[n], 1\n\t" \
    "s_cmp_eq_u32 %[n], 0\n\t" \
    "s_cbranch_scc1 2f\n\t" \
    "s_waitcnt lgkmcnt(9)\n\t" \
    "v_mfma_f32_32x32x16_bf16 v[32:47], v[68:71], v[64:67], v[32:47]\n\t" \
    "s_waitcnt lgkmcnt(0)\n\t" \
    "s_barrier\n\t" \
    "ds_read_b64_tr_b16 v[68:69], %[wa] offset:0\n\t" \
    "ds_read_b64_tr_b16 v[70:71], %[wa] offset:768\n\t" \
    "ds_read_b128 v[64:67], %[x0] offset:0\n\t" \
    "v_mfma_f32_32x32x16_bf16 v[32:47], v[80:83], v[76:79], v[32:47]\n\t" \
    "ds_read_b64_tr_b16 v[80:81], %[wa] offset:3072\n\t" \
    "ds_read_b64_tr_b16 v[82:83], %[wa] offset:3840\n\t" \
    "ds_read_b128 v[76:79], %[x1] offset:0\n\t" \
    "v_mfma_f32_32x32x16_bf16 v[32:47], v[92:95], v[88:91], v[32:47]\n\t" \
    "ds_read_b64_tr_b16 v[92:93], %[wa] offset:6144\n\t" \
    "ds_read_b64_tr_b16 v[94:95], %[wa] offset:6912\n\t" \
    "ds_read_b128 v[88:91], %[x2] offset:0\n\t" \
    "v_mfma_f32_32x32x16_bf16 v[32:47], v[104:107], v[100:103], v[32:47]\n\t" \
    "ds_read_b64_tr_b16 v[104:105], %[wa] offset:9216\n\t" \
    "ds_read_b64_tr_b16 v[106:107], %[wa] offset:9984\n\t" \
    "ds_read_b128 v[100:103], %[x3] offset:0\n\t" \
    "s_sub_u32 %[n], %[n], 1\n\t" \
    "s_cmp_eq_u32 %[n], 0\n\t" \
    "s_cbranch_scc0 1b\n\t" \
    "2:\n\t" \
    "s_waitcnt lgkmcnt(9)\n\t" \
    "v_mfma_f32_32x32x16_bf16 v[32:47], v[68:71], v[64:67], v[32:47]\n\t" \
    "s_waitcnt lgkmcnt(6)\n\t" \
    "v_mfma_f32_32x32x16_bf16 v[32:47], v[80:83], v[76:79], v[32:47]\n\t" \
    "s_cmp_eq_u32 %[kl], 2\n\t" \
    "s_cbranch_scc1 3f\n\t" \
    "s_waitcnt lgkmcnt(3)\n\t" \
    "v_mfma_f32_32x32x16_bf16 v[32:47], v[92:95], v[88:91], v[32:47]\n\t" \
    "s_waitcnt lgkmcnt(0)\n\t" \
    "v_mfma_f32_32x32x16_bf16 v[32:47], v[104:107], v[100:103], v[32:47]\n\t" \
    "3:\n\t" \
    "s_waitcnt lgkmcnt(0)\n\t" \
    "s_nop 11\n\t"
#define ASM_80T \
    "s_barrier\n\t" \
    "ds_read_b64_tr_b16 v[68:69], %[wa] offset:0\n\t" \
    "ds_read_b64_tr_b16 v[70:71], %[wa] offset:768\n\t" \
    "ds_read_b64_tr_b16 v[72:73], %[wa] offset:64\n\t" \
    "ds_read_b64_tr_b16 v[74:75], %[wa] offset:832\n\t" \
    "ds_read_b128 v[64:67], %[x0] offset:0\n\t" \
    "ds_read_b64_tr_b16 v[80:81], %[wa] offset:3072\n\t" \
    "ds_read_b64_tr_b16 v[82:83], %[wa] offset:3840\n\t" \
    "ds_read_b64_tr_b16 v[84:85], %[wa] offset:3136\n\t" \
    "ds_read_b64_tr_b16 v[86:87], %[wa] offset:3904\n\t" \
    "ds_read_b128 v[76:79], %[x0] offset:32\n\t" \
    "ds_read_b64_tr_b16 v[92:93], %[wa] offset:6144\n\t" \
    "ds_read_b64_tr_b16 v[94:95], %[wa] offset:6912\n\t" \
    "ds_read_b64_tr_b16 v[96:97], %[wa] offset:6208\n\t" \
    "ds_read_b64_tr_b16 v[98:99], %[wa] offset:6976\n\t" \
    "ds_read_b128 v[88:91], %[x0] offset:64\n\t" \
    "ds_read_b64_tr_b16 v[104:105], %[wa] offset:9216\n\t" \
    "ds_read_b64_tr_b16 v[106:107], %[wa] offset:9984\n\t" \
    "ds_read_b64_tr_b16 v[108:109], %[wa] offset:9280\n\t" \
    "ds_read_b64_tr_b16 v[110:111], %[wa] offset:10048\n\t" \
    "ds_read_b128 v[100:103], %[x0] offset:96\n\t" \
    "ds_read_b64_tr_b16 v[116:117], %[wa] offset:12288\n\t" \
    "ds_read_b64_tr_b16 v[118:119], %[wa] offset:13056\n\t" \
    "ds_read_b64_tr_b16 v[120:121], %[wa] offset:12352\n\t" \
    "ds_read_b64_tr_b16 v[122:123], %[wa] offset:13120\n\t" \
    "ds_read_b128 v[112:115], %[x0] offset:128\n\t" \
    "1:\n\t" \
    "s_waitcnt lgkmcnt(15)\n\t" \
    "v_mfma_f32_32x32x16_bf16 v[32:47], v[68:71], v[64:67], v[32:47]\n\t" \
    "v_mfma_f32_32x32x16_bf16 v[48:63], v[72:75], v[64:67], v[48:63]\n\t" \
    "s_waitcnt lgkmcnt(0)\n\t" \
    "s_barrier\n\t" \
    "ds_read_b64_tr_b16 v[68:69], %[wa] offset:26624\n\t" \
    "ds_read_b64_tr_b16 v[70:71], %[wa] offset:27392\n\t" \
    "ds_read_b64_tr_b16 v[72:73], %[wa] offset:26688\n\t" \
    "ds_read_b64_tr_b16 v[74:75], %[wa] offset:27456\n\t" \
    "ds_read_b128 v[64:67], %[x0] offset:26624\n\t" \
    "v_mfma_f32_32x32x16_bf16 v[32:47], v[80:83], v[76:79], v[32:47]\n\t" \
    "ds_read_b64_tr_b16 v[80:81], %[wa] offset:29696\n\t" \
    "ds_read_b64_tr_b16 v[82:83], %[wa] offset:30464\n\t" \
    "v_mfma_f32_32x32x16_bf16 v[48:63], v[84:87], v[76:79], v[48:63]\n\t" \
    "ds_read_b64_tr_b16 v[84:85], %[wa] offset:29760\n\t" \
    "ds_read_b64_tr_b16 v[86:87], %[wa] offset:30528\n\t" \
    "ds_read_b128 v[76:79], %[x0] offset:26656\n\t" \
    "v_mfma_f32_32x32x16_bf16 v[32:47], v[92:95], v[88:91], v[32:47]\n\t" \
    "ds_read_b64_tr_b16 v[92:93], %[wa] offset:32768\n\t" \
    "ds_read_b64_tr_b16 v[94:95], %[wa] offset:33536\n\t" \
    "v_mfma_f32_32x32x16_bf16 v[48:63], v[96:99], v[88:91], v[48:63]\n\t" \
    "ds_read_b64_tr_b16 v[96:97], %[wa] offset:32832\n\t" \
    "ds_read_b64_tr_b16 v[98:99], %[wa] offset:33600\n\t" \
    "ds_read_b128 v[88:91], %[x0] offset:26688\n\t" \
    "v_mfma_f32_32x32x16_bf16 v[32:47], v[104:107], v[100:103], v[32:47]\n\t" \
    "ds_read_b64_tr_b16 v[104:105], %[wa] offset:35840\n\t" \
    "ds_read_b64_tr_b16 v[106:107], %[wa] offset:36608\n\t" \
    "v_mfma_f32_32x32x16_bf16 v[48:63], v[108:111], v[100:103], v[48:63]\n\t" \
    "ds_read_b64_tr_b16 v[108:109], %[wa] offset:35904\n\t" \
    "ds_read_b64_tr_b16 v[110:111], %[wa] offset:36672\n\t" \
    "ds_read_b128 v[100:103], %[x0] offset:26720\n\t" \
    "v_mfma_f32_32x32x16_bf16 v[32:47], v[116:119], v[112:115], v[32:47]\n\t" \
    "ds_read_b64_tr_b16 v[116:117], %[wa] offset:38912\n\t" \
    "ds_read_b64_tr_b16 v[118:119], %[wa] offset:39680\n\t" \
    "v_mfma_f32_32x32x16_bf16 v[48:63], v[120:123], v[112:115], v[48:63]\n\t" \
    "ds_read_b64_tr_b16 v[120:121], %[wa] offset:38976\n\t" \
    "ds_read_b64_tr_b16 v[122:123], %[wa] offset:39744\n\t" \
    "ds_read_b128 v[112:115], %[x0] offset:26752\n\t" \
    "s_sub_u32 %[n], %[n], 1\n\t" \
    "s_cmp_eq_u32 %[n], 0\n\t" \
    "s_cbranch_scc1 2f\n\t" \
    "s_waitcnt lgkmcnt(15)\n\t" \
    "v_mfma_f32_32x32x16_bf16 v[32:47], v[68:71], v[64:67], v[32:47]\n\t" \
    "v_mfma_f32_32x32x16_bf16 v[48:63], v[72:75], v[64:67], v[48:63]\n\t" \
    "s_waitcnt lgkmcnt(0)\n\t" \
    "s_barrier\n\t" \
    "ds_read_b64_tr_b16 v[68:69], %[wa] offset:53248\n\t" \
    "ds_read_b64_tr_b16 v[70:71], %[wa] offset:54016\n\t" \
    "ds_read_b64_tr_b16 v[72:73], %[wa] offset:53312\n\t" \
    "ds_read_b64_tr_b16 v[74:75], %[wa] offset:54080\n\t" \
    "ds_read_b128 v[64:67], %[x0] offset:53248\n\t" \
    "v_mfma_f32_32x32x16_bf16 v[32:47], v[80:83], v[76:79], v[32:47]\n\t" \
    "ds_read_b64_tr_b16 v[80:81], %[wa] offset:56320\n\t" \
    "ds_read_b64_tr_b16 v[82:83], %[wa] offset:57088\n\t" \
    "v_mfma_f32_32x32x16_bf16 v[48:63], v[84:87], v[76:79], v[48:63]\n\t" \
    "ds_read_b64_tr_b16 v[84:85], %[wa] offset:56384\n\t" \
    "ds_read_b64_tr_b16 v[86:87], %[wa] offset:57152\n\t" \
    "ds_read_b128 v[76:79], %[x0] offset:53280\n\t" \
    "v_mfma_f32_32x32x16_bf16 v[32:47], v[92:95], v[88:91], v[32:47]\n\t" \
    "ds_read_b64_tr_b16 v[92:93], %[wa] offset:59392\n\t" \
    "ds_read_b64_tr_b16 v[94:95], %[wa] offset:60160\n\t" \
    "v_mfma_f32_32x32x16_bf16 v[48:63], v[96:99], v[88:91], v[48:63]\n\t" \
    "ds_read_b64_tr_b16 v[96:97], %[wa] offset:59456\n\t" \
    "ds_read_b64_tr_b16 v[98:99], %[wa] offset:60224\n\t" \
    "ds_read_b128 v[88:91], %[x0] offset:53312\n\t" \
    "v_mfma_f32_32x32x16_bf16 v[32:47], v[104:107], v[100:103], v[32:47]\n\t" \
    "ds_read_b64_tr_b16 v[104:105], %[wa] offset:62464\n\t" \
    "ds_read_b64_tr_b16 v[106:107], %[wa] offset:63232\n\t" \
    "v_mfma_f32_32x32x16_bf16 v[48:63], v[108:111], v[100:103], v[48:63]\n\t" \
    "ds_read_b64_tr_b16 v[108:109], %[wa] offset:62528\n\t" \
    "ds_read_b64_tr_b16 v[110:111], %[wa] offset:63296\n\t" \
    "ds_read_b128 v[100:103], %[x0] offset:53344\n\t" \
    "v_mfma_f32_32x32x16_bf16 v[32:47], v[116:119], v[112:115], v[32:47]\n\t" \
    "ds_read_b64_tr_b16 v[116:117], %[wc] offset:12288\n\t" \
    "ds_read_b64_tr_b16 v[118:119], %[wc] offset:13056\n\t" \
    "v_mfma_f32_32x32x16_bf16 v[48:63], v[120:123], v[112:115], v[48:63]\n\t" \
    "ds_read_b64_tr_b16 v[120:121], %[wc] offset:12352\n\t" \
    "ds_read_b64_tr_b16 v[122:123], %[wc] offset:13120\n\t" \
    "ds_read_b128 v[112:115], %[x0] offset:53376\n\t" \
    "s_sub_u32 %[n], %[n], 1\n\t" \
    "s_cmp_eq_u32 %[n], 0\n\t" \
    "s_cbranch_scc1 2f\n\t" \
    "s_waitcnt lgkmcnt(15)\n\t" \
    "v_mfma_f32_32x32x16_bf16 v[32:47], v[68:71], v[64:67], v[32:47]\n\t" \
    "v_mfma_f32_32x32x16_bf16 v[48:63], v[72:75], v[64:67], v[48:63]\n\t" \
    "s_waitcnt lgkmcnt(0)\n\t" \
    "s_barrier\n\t" \
    "ds_read_b64_tr_b16 v[68:69], %[wa] offset:0\n\t" \
    "ds_read_b64_tr_b16 v[70:71], %[wa] offset:768\n\t" \
    "ds_read_b64_tr_b16 v[72:73], %[wa] offset:64\n\t" \
    "ds_read_b64_tr_b16 v[74:75], %[wa] offset:832\n\t" \
    "ds_read_b128 v[64:67], %[x0] offset:0\n\t" \
    "v_mfma_f32_32x32x16_bf16 v[32:47], v[80:83], v[76:79], v[32:47]\n\t" \
    "ds_read_b64_tr_b16 v[80:81], %[wa] offset:3072\n\t" \
    "ds_read_b64_tr_b16 v[82:83], %[wa] offset:3840\n\t" \
    "v_mfma_f32_32x32x16_bf16 v[48:63], v[84:87], v[76:79], v[48:63]\n\t" \
    "ds_read_b64_tr_b16 v[84:85], %[wa] offset:3136\n\t" \
    "ds_read_b64_tr_b16 v[86:87], %[wa] offset:3904\n\t" \
    "ds_read_b128 v[76:79], %[x0] offset:32\n\t" \
    "v_mfma_f32_32x32x16_bf16 v[32:47], v[92:95], v[88:91], v[32:47]\n\t" \
    "ds_read_b64_tr_b16 v[92:93], %[wa] offset:6144\n\t" \
    "ds_read_b64_tr_b16 v[94:95], %[wa] offset:6912\n\t" \
    "v_mfma_f32_32x32x16_bf16 v[48:63], v[96:99], v[88:91], v[48:63]\n\t" \
    "ds_read_b64_tr_b16 v[96:97], %[wa] offset:6208\n\t" \
    "ds_read_b64_tr_b16 v[98:99], %[wa] offset:6976\n\t" \
    "ds_read_b128 v[88:91], %[x0] offset:64\n\t" \
    "v_mfma_f32_32x32x16_bf16 v[32:47], v[104:107], v[100:103], v[32:47]\n\t" \
    "ds_read_b64_tr_b16 v[104:105], %[wa] offset:9216\n\t" \
    "ds_read_b64_tr_b16 v[106:107], %[wa] offset:9984\n\t" \
    "v_mfma_f32_32x32x16_bf16 v[48:63], v[108:111], v[100:103], v[48:63]\n\t" \
    "ds_read_b64_tr_b16 v[108:109], %[wa] offset:9280\n\t" \
    "ds_read_b64_tr_b16 v[110:111], %[wa] offset:10048\n\t" \
    "ds_read_b128 v[100:103], %[x0] offset:96\n\t" \
    "v_mfma_f32_32x32x16_bf16 v[32:47], v[116:119], v[112:115], v[32:47]\n\t" \
    "ds_read_b64_tr_b16 v[116:117], %[wa] offset:12288\n\t" \
    "ds_read_b64_tr_b16 v[118:119], %[wa] offset:13056\n\t" \
    "v_mfma_f32_32x32x16_bf16 v[48:63], v[120:123], v[112:115], v[48:63]\n\t" \
    "ds_read_b64_tr_b16 v[120:121], %[wa] offset:12352\n\t" \
    "ds_read_b64_tr_b16 v[122:123], %[wa] offset:13120\n\t" \
    "ds_read_b128 v[112:115], %[x0] offset:128\n\t" \
    "s_sub_u32 %[n], %[n], 1\n\t" \
    "s_cmp_eq_u32 %[n], 0\n\t" \
    "s_cbranch_scc0 1b\n\t" \
    "2:\n\t" \
    "s_waitcnt lgkmcnt(15)\n\t" \
    "v_mfma_f32_32x32x16_bf16 v[32:47], v[68:71], v[64:67], v[32:47]\n\t" \
    "v_mfma_f32_32x32x16_bf16 v[48:63], v[72:75], v[64:67], v[48:63]\n\t" \
    "s_waitcnt lgkmcnt(15)\n\t" \
    "v_mfma_f32_32x32x16_bf16 v[32:47], v[80:83], v[76:79], v[32:47]\n\t" \
    "v_mfma_f32_32x32x16_bf16 v[48:63], v[84:87], v[76:79], v[48:63]\n\t" \
    "s_waitcnt lgkmcnt(10)\n\t" \
    "v_mfma_f32_32x32x16_bf16 v[32:47], v[92:95], v[88:91], v[32:47]\n\t" \
    "v_mfma_f32_32x32x16_bf16 v[48:63], v[96:99], v[88:91], v[48:63]\n\t" \
    "s_waitcnt lgkmcnt(5)\n\t" \
    "v_mfma_f32_32x32x16_bf16 v[32:47], v[104:107], v[100:103], v[32:47]\n\t" \
    "v_mfma_f32_32x32x16_bf16 v[48:63], v[108:111], v[100:103], v[48:63]\n\t" \
    "s_waitcnt lgkmcnt(0)\n\t" \
    "v_mfma_f32_32x32x16_bf16 v[32:47], v[116:119], v[112:115], v[32:47]\n\t" \
    "v_mfma_f32_32x32x16_bf16 v[48:63], v[120:123], v[112:115], v[48:63]\n\t" \
    "3:\n\t" \
    "s_waitcnt lgkmcnt(0)\n\t" \
    "s_nop 11\n\t"
#define ASM_80S \
    "s_barrier\n\t" \
    "ds_read_b64_tr_b16 v[68:69], %[wa] offset:0\n\t" \
    "ds_read_b64_tr_b16 v[70:71], %[wa] offset:768\n\t" \
    "ds_read_b128 v[64:67], %[x0] offset:0\n\t" \
    "ds_read_b64_tr_b16 v[80:81], %[wa] offset:3072\n\t" \
    "ds_read_b64_tr_b16 v[82:83], %[wa] offset:3840\n\t" \
    "ds_read_b128 v[76:79], %[x0] offset:32\n\t" \
    "ds_read_b64_tr_b16 v[92:93], %[wa] offset:6144\n\t" \
    "ds_read_b64_tr_b16 v[94:95], %[wa] offset:6912\n\t" \
    "ds_read_b128 v[88:91], %[x0] offset:64\n\t" \
    "ds_read_b64_tr_b16 v[104:105], %[wa] offset:9216\n\t" \
    "ds_read_b64_tr_b16 v[106:107], %[wa] offset:9984\n\t" \
    "ds_read_b128 v[100:103], %[x0] offset:96\n\t" \
    "ds_read_b64_tr_b16 v[116:117], %[wa] offset:12288\n\t" \
    "ds_read_b64_tr_b16 v[118:119], %[wa] offset:13056\n\t" \
    "ds_read_b128 v[112:115], %[x0] offset:128\n\t" \
    "1:\n\t" \
    "s_waitcnt lgkmcnt(12)\n\t" \
    "v_mfma_f32_32x32x16_bf16 v[32:47], v[68:71], v[64:67], v[32:47]\n\t" \
    "s_waitcnt lgkmcnt(0)\n\t" \
    "s_barrier\n\t" \
    "ds_read_b64_tr_b16 v[68:69], %[wa] offset:26624\n\t" \
    "ds_read_b64_tr_b16 v[70:71], %[wa] offset:27392\n\t" \
    "ds_read_b128 v[64:67], %[x0] offset:26624\n\t" \
    "v_mfma_f32_32x32x16_bf16 v[32:47], v[80:83], v[76:79], v[32:47]\n\t" \
    "ds_read_b64_tr_b16 v[80:81], %[wa] offset:29696\n\t" \
    "ds_read_b64_tr_b16 v[82:83], %[wa] offset:30464\n\t" \
    "ds_read_b128 v[76:79], %[x0] offset:26656\n\t" \
    "v_mfma_f32_32x32x16_bf16 v[32:47], v[92:95], v[88:91], v[32:47]\n\t" \
    "ds_read_b64_tr_b16 v[92:93], %[wa] offset:32768\n\t" \
    "ds_read_b64_tr_b16 v[94:95], %[wa] offset:33536\n\t" \
    "ds_read_b128 v[88:91], %[x0] offset:26688\n\t" \
    "v_mfma_f32_32x32x16_bf16 v[32:47], v[104:107], v[100:103], v[32:47]\n\t" \
    "ds_read_b64_tr_b16 v[104:105], %[wa] offset:35840\n\t" \
    "ds_read_b64_tr_b16 v[106:107], %[wa] offset:36608\n\t" \
    "ds_read_b128 v[100:103], %[x0] offset:26720\n\t" \
    "v_mfma_f32_32x32x16_bf16 v[32:47], v[116:119], v[112:115], v[32:47]\n\t" \
    "ds_read_b64_tr_b16 v[116:117], %[wa] offset:38912\n\t" \
    "ds_read_b64_tr_b16 v[118:119], %[wa] offset:39680\n\t" \
    "ds_read_b128 v[112:115], %[x0] offset:26752\n\t" \
    "s_sub_u32 %[n], %[n], 1\n\t" \
    "s_cmp_eq_u32 %[n], 0\n\t" \
    "s_cbranch_scc1 2f\n\t" \
    "s_waitcnt lgkmcnt(12)\n\t" \
    "v_mfma_f32_32x32x16_bf16 v[32:47], v[68:71], v[64:67], v[32:47]\n\t" \
    "s_waitcnt lgkmcnt(0)\n\t" \
    "s_barrier\n\t" \
    "ds_read_b64_tr_b16 v[68:69], %[wa] offset:53248\n\t" \
    "ds_read_b64_tr_b16 v[70:71], %[wa] offset:54016\n\t" \
    "ds_read_b128 v[64:67], %[x0] offset:53248\n\t" \
    "v_mfma_f32_32x32x16_bf16 v[32:47], v[80:83], v[76:79], v[32:47]\n\t" \
    "ds_read_b64_tr_b16 v[80:81], %[wa] offset:56320\n\t" \
    "ds_read_b64_tr_b16 v[82:83], %[wa] offset:57088\n\t" \
    "ds_read_b128 v[76:79], %[x0] offset:53280\n\t" \
    "v_mfma_f32_32x32x16_bf16 v[32:47], v[92:95], v[88:91], v[32:47]\n\t" \
    "ds_read_b64_tr_b16 v[92:93], %[wa] offset:59392\n\t" \
    "ds_read_b64_tr_b16 v[94:95], %[wa] offset:60160\n\t" \
    "ds_read_b128 v[88:91], %[x0] offset:53312\n\t" \
    "v_mfma_f32_32x32x16_bf16 v[32:47], v[104:107], v[100:103], v[32:47]\n\t" \
    "ds_read_b64_tr_b16 v[104:105], %[wa] offset:62464\n\t" \
    "ds_read_b64_tr_b16 v[106:107], %[wa] offset:63232\n\t" \
    "ds_read_b128 v[100:103], %[x0] offset:53344\n\t" \
    "v_mfma_f32_32x32x16_bf16 v[32:47], v[116:119], v[112:115], v[32:47]\n\t" \
    "ds_read_b64_tr_b16 v[116:117], %[wc] offset:12288\n\t" \
    "ds_read_b64_tr_b16 v[118:119], %[wc] offset:13056\n\t" \
    "ds_read_b128 v[112:115], %[x0] offset:53376\n\t" \
    "s_sub_u32 %[n], %[n], 1\n\t" \
    "s_cmp_eq_u32 %[n], 0\n\t" \
    "s_cbranch_scc1 2f\n\t" \
    "s_waitcnt lgkmcnt(12)\n\t" \
    "v_mfma_f32_32x32x16_bf16 v[32:47], v[68:71], v[64:67], v[32:47]\n\t" \
    "s_waitcnt lgkmcnt(0)\n\t" \
    "s_barrier\n\t" \
    "ds_read_b64_tr_b16 v[68:69], %[wa] offset:0\n\t" \
    "ds_read_b64_tr_b16 v[70:71], %[wa] offset:768\n\t" \
    "ds_read_b128 v[64:67], %[x0] offset:0\n\t" \
    "v_mfma_f32_32x32x16_bf16 v[32:47], v[80:83], v[76:79], v[32:47]\n\t" \
    "ds_read_b64_tr_b16 v[80:81], %[wa] offset:3072\n\t" \
    "ds_read_b64_tr_b16 v[82:83], %[wa] offset:3840\n\t" \
    "ds_read_b128 v[76:79], %[x0] offset:32\n\t" \
    "v_mfma_f32_32x32x16_bf16 v[32:47], v[92:95], v[88:91], v[32:47]\n\t" \
    "ds_read_b64_tr_b16 v[92:93], %[wa] offset:6144\n\t" \
    "ds_read_b64_tr_b16 v[94:95], %[wa] offset:6912\n\t" \
    "ds_read_b128 v[88:91], %[x0] offset:64\n\t" \
    "v_mfma_f32_32x32x16_bf16 v[32:47], v[104:107], v[100:103], v[32:47]\n\t" \
    "ds_read_b64_tr_b16 v[104:105], %[wa] offset:9216\n\t" \
    "ds_read_b64_tr_b16 v[106:107], %[wa] offset:9984\n\t" \
    "ds_read_b128 v[100:103], %[x0] offset:96\n\t" \
    "v_mfma_f32_32x32x16_bf16 v[32:47], v[116:119], v[112:115], v[32:47]\n\t" \
    "ds_read_b64_tr_b16 v[116:117], %[wa] offset:12288\n\t" \
    "ds_read_b64_tr_b16 v[118:119], %[wa] offset:13056\n\t" \
    "ds_read_b128 v[112:115], %[x0] offset:128\n\t" \
    "s_sub_u32 %[n], %[n], 1\n\t" \
    "s_cmp_eq_u32 %[n], 0\n\t" \
    "s_cbranch_scc0 1b\n\t" \
    "2:\n\t" \
    "s_waitcnt lgkmcnt(12)\n\t" \
    "v_mfma_f32_32x32x16_bf16 v[32:47], v[68:71], v[64:67], v[32:47]\n\t" \
    "s_waitcnt lgkmcnt(9)\n\t" \
    "v_mfma_f32_32x32x16_bf16 v[32:47], v[80:83], v[76:79], v[32:47]\n\t" \
    "s_waitcnt lgkmcnt(6)\n\t" \
    "v_mfma_f32_32x32x16_bf16 v[32:47], v[92:95], v[88:91], v[32:47]\n\t" \
    "s_waitcnt lgkmcnt(3)\n\t" \
    "v_mfma_f32_32x32x16_bf16 v[32:47], v[104:107], v[100:103], v[32:47]\n\t" \
    "s_waitcnt lgkmcnt(0)\n\t" \
    "v_mfma_f32_32x32x16_bf16 v[32:47], v[116:119], v[112:115], v[32:47]\n\t" \
    "3:\n\t" \
    "s_waitcnt lgkmcnt(0)\n\t" \
    "s_nop 11\n\t"
#define CLOB_F48 "v64", "v65", "v66", "v67", "v68", "v69", "v70", "v71", "v72", "v73", "v74", "v75", "v76", "v77", "v78", "v79", "v80", "v81", "v82", "v83", "v84", "v85", "v86", "v87", "v88", "v89", "v90", "v91", "v92", "v93", "v94", "v95", "v96", "v97", "v98", "v99", "v100", "v101", "v102", "v103", "v104", "v105", "v106", "v107", "v108", "v109", "v110", "v111"
#define CLOB_F60 "v64", "v65", "v66", "v67", "v68", "v69", "v70", "v71", "v72", "v73", "v74", "v75", "v76", "v77", "v78", "v79", "v80", "v81", "v82", "v83", "v84", "v85", "v86", "v87", "v88", "v89", "v90", "v91", "v92", "v93", "v94", "v95", "v96", "v97", "v98", "v99", "v100", "v101", "v102", "v103", "v104", "v105", "v106", "v107", "v108", "v109", "v110", "v111", "v112", "v113", "v114", "v115", "v116", "v117", "v118", "v119", "v120", "v121", "v122", "v123"
template <int PA, int PB, class F, class H> DI void load_loop(int nst, int lw, F&& issue, H&& idle TLARG) {
#pragma unroll 1
    for (int t = 0; t < nst; ++t) {
        if (t >= 3) { if (PA == PB || lw < 2) waitv_bar<PA>(); else waitv_bar<PB>(); }
        TL(tb + t);
        issue(t);
        if (t == 2) { idle(); if (PA == PB || lw < 2) waitv_bar<2 * PA>(); else waitv_bar<2 * PB>(); }
    }
    if (PA == PB || lw < 2) waitv_bar<PA>(); else waitv_bar<PB>();
    waitv_bar<0>();
    TL(tb + nst);
}
DI void bar_only() { asm volatile("s_barrier" ::: "memory"); }
constexpr int SPARE = 73728;
DI void bars(int n) {
#pragma unroll 1
    for (int i = 0; i < n; ++i) bar_only();
}
template <bool SC1> DI void pull_l2(const char* src, int first, int step, int n, unsigned sink, int lane) {
#pragma unroll 1
    for (int p = first; p < n; p += step) glds16<SC1>(src, (unsigned)(p * 1024 + lane * 16), sink);
}
constexpr int G128 = 24576;
constexpr int G96 = 20480;
constexpr int G80 = 26624;

struct Addr128 { unsigned x0, x1, x2, x3, a, b; };
DI Addr128 addr128(unsigned lds0, int w, int lane) {
    const int r32 = lane & 31, h = lane >> 5, q = (lane & 15) >> 2, p = lane & 3, tm = w & 1, tn2 = w >> 1;
    Addr128 r;
    const unsigned xr = lds0 + (unsigned)((32 * tm + r32) * 128), xs = (unsigned)(h ^ ((r32 >> 1) & 7));
    r.x0 = xr + (xs << 4); r.x1 = xr + ((xs ^ 2) << 4); r.x2 = xr + ((xs ^ 4) << 4); r.x3 = xr + ((xs ^ 6) << 4);
    const unsigned wr = lds0 + (unsigned)(8192 + (8 * h + q) * 256 + (p & 1) * 8), c0 = (unsigned)(8 * tn2 + 2 * ((lane >> 4) & 1) + (p >> 1));
    r.a = wr + ((c0 ^ (q << 2)) << 4);
    r.b = wr + (((c0 + 4) ^ (q << 2)) << 4);
    return r;
}
#define ADDR128_OPS(ad) [x0] "v"(ad.x0), [x1] "v"(ad.x1), [x2] "v"(ad.x2), [x3] "v"(ad.x3), [wa] "v"(ad.a), [wb] "v"(ad.b)
DI void comp96(f32x16& a0, f32x16& a1, int n, int kl, unsigned lds0, int w, int lane) {
    const int r32 = lane & 31, h = lane >> 5, q = (lane & 15) >> 2, p = lane & 3;
    const unsigned xr = lds0 + (unsigned)((32 * (w & 1) + r32) * 128), xs = (unsigned)(h ^ ((r32 >> 1) & 7));
    const unsigned x0 = xr + (xs << 4), x1 = xr + ((xs ^ 2) << 4), x2 = xr + ((xs ^ 4) << 4), x3 = xr + ((xs ^ 6) << 4);
    const unsigned wa = lds0 + (unsigned)(8192 + (8 * h + q) * 192 + (w & 2) * 64 + ((lane >> 4) & 1) * 32 + p * 8);
    if (w < 2)
        asm volatile(ASM_96T : "+{v[32:47]}"(a0), "+{v[48:63]}"(a1), [n] "+s"(n)
                     : [x0] "v"(x0), [x1] "v"(x1), [x2] "v"(x2), [x3] "v"(x3), [wa] "v"(wa), [kl] "s"(kl) : CLOB_F48, "scc", "memory");
    else
        asm volatile(ASM_96S : "+{v[32:47]}"(a0), [n] "+s"(n)
                     : [x0] "v"(x0), [x1] "v"(x1), [x2] "v"(x2), [x3] "v"(x3), [wa] "v"(wa), [kl] "s"(kl) : CLOB_F48, "scc", "memory");
}
DI void comp80(f32x16& a0, f32x16& a1, int n, unsigned lds0, int w, int lane) {
    const int r32 = lane & 31, h = lane >> 5, q = (lane & 15) >> 2, p = lane & 3;
    const unsigned x0 = lds0 + (unsigned)((32 * (w & 1) + r32) * 176 + h * 16);
    const unsigned wa = lds0 + (unsigned)(OBLOB + (8 * h + q) * 192 + (w & 2) * 64 + ((lane >> 4) & 1) * 32 + p * 8), wc = wa + 2 * G80;
    if (w < 2)
        asm volatile(ASM_80T : "+{v[32:47]}"(a0), "+{v[48:63]}"(a1), [n] "+s"(n) : [x0] "v"(x0), [wa] "v"(wa), [wc] "v"(wc) : CLOB_F60, "scc", "memory");
    else
        asm volatile(ASM_80S : "+{v[32:47]}"(a0), [n] "+s"(n) : [x0] "v"(x0), [wa] "v"(wa), [wc] "v"(wc) : CLOB_F60, "scc", "memory");
}
DI unsigned xsrc_off(int pc, int lane, int ld2) { return (unsigned)((8 * pc + (lane >> 3)) * ld2 + (((lane & 7) ^ ((4 * pc + (lane >> 4)) & 7)) << 4)); }
DI unsigned w96_off(int pc, int lane, int maxc, int maxrow) {
    const int c = pc * 64 + lane, row = c / 12, pos = c - row * 12;
    return (unsigned)(min(row, maxrow) * DM * 2 + min(pos, maxc) * 16);
}

#define APRIO 2
#define CPRIO 1
#define ENGINE 1
#define LATE 30

#define ATTN_LOOP_ASM \
    "ds_read_b128 v[112:115], v28\n\t" \
    "ds_read_b64 v[124:125], v29\n\t" \
    "v_mov_b32 v104, 0\n\t" \
    "v_mov_b32 v105, 0\n\t" \
    "v_mov_b32 v106, 0\n\t" \
    "v_mov_b32 v107, 0\n\t" \
    "v_mov_b32 v108, 0\n\t" \
    "v_mov_b32 v109, 0\n\t" \
    "v_mov_b32 v110, 0\n\t" \
    "v_mov_b32 v111, 0\n\t" \
    "v_mov_b32 v116, 0\n\t" \
    "v_mov_b32 v117, 0\n\t" \
    "v_mov_b32 v118, 0\n\t" \
    "v_mov_b32 v119, 0\n\t" \
    "v_mov_b32 v120, 0\n\t" \
    "v_mov_b32 v121, 0\n\t" \
    "v_mov_b32 v122, 0\n\t" \
    "v_mov_b32 v123, 0\n\t" \
    "v_mov_b32 v32, 0\n\t" \
    "v_mov_b32 v33, 0\n\t" \
    "v_mov_b32 v34, 0\n\t" \
    "v_mov_b32 v35, 0\n\t" \
    "v_mov_b32 v36, 0\n\t" \
    "v_mov_b32 v37, 0\n\t" \
    "v_mov_b32 v38, 0\n\t" \
    "v_mov_b32 v39, 0\n\t" \
    "v_mov_b32 v40, 0\n\t" \
    "v_mov_b32 v41, 0\n\t" \
    "v_mov_b32 v42, 0\n\t" \
    "v_mov_b32 v43, 0\n\t" \
    "v_mov_b32 v44, 0\n\t" \
    "v_mov_b32 v45, 0\n\t" \
    "v_mov_b32 v46, 0\n\t" \
    "v_mov_b32 v47, 0\n\t" \
    "v_mov_b32 v48, 0\n\t" \
    "v_mov_b32 v49, 0\n\t" \
    "v_mov_b32 v50, 0\n\t" \
    "v_mov_b32 v51, 0\n\t" \
    "v_mov_b32 v52, 0\n\t" \
    "v_mov_b32 v53, 0\n\t" \
    "v_mov_b32 v54, 0\n\t" \
    "v_mov_b32 v55, 0\n\t" \
    "v_mov_b32 v56, 0\n\t" \
    "v_mov_b32 v57, 0\n\t" \
    "v_mov_b32 v58, 0\n\t" \
    "v_mov_b32 v59, 0\n\t" \
    "v_mov_b32 v60, 0\n\t" \
    "v_mov_b32 v61, 0\n\t" \
    "v_mov_b32 v62, 0\n\t" \
    "v_mov_b32 v63, 0\n\t" \
    "s_mov_b32 %[cnt], 0\n\t" \
    "s_mov_b32 %[ph], 0\n\t" \
    "s_waitcnt lgkmcnt(0)\n\t" \
    "v_mfma_f32_32x32x16_bf16 v[64:79], v[112:115], v[16:19], 0\n\t" \
    "v_mfma_f32_32x32x8_bf16 v[64:79], v[124:125], v[24:25], v[64:79]\n\t" \
    "s_nop 11\n\t" \
    "1:\n\t" \
    "v_mfma_f32_32x32x16_bf16 v[80:95], v[112:115], v[20:23], 0\n\t" \
    "v_exp_f32 v64, v64\n\t" \
    "v_exp_f32 v65, v65\n\t" \
    "v_exp_f32 v66, v66\n\t" \
    "v_exp_f32 v67, v67\n\t" \
    "v_cvt_pk_bf16_f32 v96, v64, v65\n\t" \
    "v_cvt_pk_bf16_f32 v97, v66, v67\n\t" \
    "v_mfma_f32_32x32x8_bf16 v[80:95], v[124:125], v[26:27], v[80:95]\n\t" \
    "ds_read_b128 v[112:115], v28 offset:3072\n\t" \
    "ds_read_b64 v[124:125], v29 offset:3072\n\t" \
    "v_exp_f32 v68, v68\n\t" \
    "v_exp_f32 v69, v69\n\t" \
    "v_exp_f32 v70, v70\n\t" \
    "v_exp_f32 v71, v71\n\t" \
    "v_cvt_pk_bf16_f32 v98, v68, v69\n\t" \
    "v_cvt_pk_bf16_f32 v99, v70, v71\n\t" \
    "v_mfma_f32_32x32x16_bf16 v[48:63], v[116:119], v[104:107], v[48:63]\n\t" \
    "ds_read_b128 v[116:119], v30 offset:0\n\t" \
    "v_exp_f32 v72, v72\n\t" \
    "v_exp_f32 v73, v73\n\t" \
    "v_exp_f32 v74, v74\n\t" \
    "v_exp_f32 v75, v75\n\t" \
    "v_cvt_pk_bf16_f32 v100, v72, v73\n\t" \
    "v_cvt_pk_bf16_f32 v101, v74, v75\n\t" \
    "v_mfma_f32_32x32x16_bf16 v[48:63], v[120:123], v[108:111], v[48:63]\n\t" \
    "ds_read_b128 v[120:123], v30 offset:768\n\t" \
    "v_exp_f32 v76, v76\n\t" \
    "v_exp_f32 v77, v77\n\t" \
    "v_exp_f32 v78, v78\n\t" \
    "v_exp_f32 v79, v79\n\t" \
    "v_cvt_pk_bf16_f32 v102, v76, v77\n\t" \
    "v_cvt_pk_bf16_f32 v103, v78, v79\n\t" \
    "s_waitcnt lgkmcnt(3)\n\t" \
    "v_mfma_f32_32x32x16_bf16 v[64:79], v[112:115], v[16:19], 0\n\t" \
    "v_exp_f32 v80, v80\n\t" \
    "v_exp_f32 v81, v81\n\t" \
    "v_exp_f32 v82, v82\n\t" \
    "v_exp_f32 v83, v83\n\t" \
    "v_cvt_pk_bf16_f32 v104, v80, v81\n\t" \
    "v_cvt_pk_bf16_f32 v105, v82, v83\n\t" \
    "s_waitcnt lgkmcnt(2)\n\t" \
    "v_mfma_f32_32x32x8_bf16 v[64:79], v[124:125], v[24:25], v[64:79]\n\t" \
    "v_exp_f32 v84, v84\n\t" \
    "v_exp_f32 v85, v85\n\t" \
    "v_exp_f32 v86, v86\n\t" \
    "v_exp_f32 v87, v87\n\t" \
    "v_cvt_pk_bf16_f32 v106, v84, v85\n\t" \
    "v_cvt_pk_bf16_f32 v107, v86, v87\n\t" \
    "s_waitcnt lgkmcnt(1)\n\t" \
    "v_mfma_f32_32x32x16_bf16 v[32:47], v[116:119], v[96:99], v[32:47]\n\t" \
    "v_exp_f32 v88, v88\n\t" \
    "v_exp_f32 v89, v89\n\t" \
    "v_exp_f32 v90, v90\n\t" \
    "v_exp_f32 v91, v91\n\t" \
    "v_cvt_pk_bf16_f32 v108, v88, v89\n\t" \
    "v_cvt_pk_bf16_f32 v109, v90, v91\n\t" \
    "s_waitcnt lgkmcnt(0)\n\t" \
    "v_mfma_f32_32x32x16_bf16 v[32:47], v[120:123], v[100:103], v[32:47]\n\t" \
    "v_exp_f32 v92, v92\n\t" \
    "v_exp_f32 v93, v93\n\t" \
    "v_exp_f32 v94, v94\n\t" \
    "v_exp_f32 v95, v95\n\t" \
    "v_cvt_pk_bf16_f32 v110, v92, v93\n\t" \
    "v_cvt_pk_bf16_f32 v111, v94, v95\n\t" \
    "v_mfma_f32_32x32x16_bf16 v[80:95], v[112:115], v[20:23], 0\n\t" \
    "v_exp_f32 v64, v64\n\t" \
    "v_exp_f32 v65, v65\n\t" \
    "v_exp_f32 v66, v66\n\t" \
    "v_exp_f32 v67, v67\n\t" \
    "v_cvt_pk_bf16_f32 v96, v64, v65\n\t" \
    "v_cvt_pk_bf16_f32 v97, v66, v67\n\t" \
    "v_mfma_f32_32x32x8_bf16 v[80:95], v[124:125], v[26:27], v[80:95]\n\t" \
    "ds_read_b128 v[112:115], v28 offset:6144\n\t" \
    "ds_read_b64 v[124:125], v29 offset:6144\n\t" \
    "v_exp_f32 v68, v68\n\t" \
    "v_exp_f32 v69, v69\n\t" \
    "v_exp_f32 v70, v70\n\t" \
    "v_exp_f32 v71, v71\n\t" \
    "v_cvt_pk_bf16_f32 v98, v68, v69\n\t" \
    "v_cvt_pk_bf16_f32 v99, v70, v71\n\t" \
    "v_mfma_f32_32x32x16_bf16 v[48:63], v[116:119], v[104:107], v[48:63]\n\t" \
    "ds_read_b128 v[116:119], v30 offset:3072\n\t" \
    "v_exp_f32 v72, v72\n\t" \
    "v_exp_f32 v73, v73\n\t" \
    "v_exp_f32 v74, v74\n\t" \
    "v_exp_f32 v75, v75\n\t" \
    "v_cvt_pk_bf16_f32 v100, v72, v73\n\t" \
    "v_cvt_pk_bf16_f32 v101, v74, v75\n\t" \
    "v_mfma_f32_32x32x16_bf16 v[48:63], v[120:123], v[108:111], v[48:63]\n\t" \
    "ds_read_b128 v[120:123], v30 offset:3840\n\t" \
    "v_exp_f32 v76, v76\n\t" \
    "v_exp_f32 v77, v77\n\t" \
    "v_exp_f32 v78, v78\n\t" \
    "v_exp_f32 v79, v79\n\t" \
    "v_cvt_pk_bf16_f32 v102, v76, v77\n\t" \
    "v_cvt_pk_bf16_f32 v103, v78, v79\n\t" \
    "s_waitcnt lgkmcnt(3)\n\t" \
    "v_mfma_f32_32x32x16_bf16 v[64:79], v[112:115], v[16:19], 0\n\t" \
    "v_exp_f32 v80, v80\n\t" \
    "v_exp_f32 v81, v81\n\t" \
    "v_exp_f32 v82, v82\n\t" \
    "v_exp_f32 v83, v83\n\t" \
    "v_cvt_pk_bf16_f32 v104, v80, v81\n\t" \
    "v_cvt_pk_bf16_f32 v105, v82, v83\n\t" \
    "s_waitcnt lgkmcnt(2)\n\t" \
    "v_mfma_f32_32x32x8_bf16 v[64:79], v[124:125], v[24:25], v[64:79]\n\t" \
    "v_exp_f32 v84, v84\n\t" \
    "v_exp_f32 v85, v85\n\t" \
    "v_exp_f32 v86, v86\n\t" \
    "v_exp_f32 v87, v87\n\t" \
    "v_cvt_pk_bf16_f32 v106, v84, v85\n\t" \
    "v_cvt_pk_bf16_f32 v107, v86, v87\n\t" \
    "s_waitcnt lgkmcnt(1)\n\t" \
    "v_mfma_f32_32x32x16_bf16 v[32:47], v[116:119], v[96:99], v[32:47]\n\t" \
    "v_exp_f32 v88, v88\n\t" \
    "v_exp_f32 v89, v89\n\t" \
    "v_exp_f32 v90, v90\n\t" \
    "v_exp_f32 v91, v91\n\t" \
    "v_cvt_pk_bf16_f32 v108, v88, v89\n\t" \
    "v_cvt_pk_bf16_f32 v109, v90, v91\n\t" \
    "s_waitcnt lgkmcnt(0)\n\t" \
    "v_mfma_f32_32x32x16_bf16 v[32:47], v[120:123], v[100:103], v[32:47]\n\t" \
    "v_exp_f32 v92, v92\n\t" \
    "v_exp_f32 v93, v93\n\t" \
    "v_exp_f32 v94, v94\n\t" \
    "v_exp_f32 v95, v95\n\t" \
    "v_cvt_pk_bf16_f32 v110, v92, v93\n\t" \
    "v_cvt_pk_bf16_f32 v111, v94, v95\n\t" \
    "v_mfma_f32_32x32x16_bf16 v[80:95], v[112:115], v[20:23], 0\n\t" \
    "v_exp_f32 v64, v64\n\t" \
    "v_exp_f32 v65, v65\n\t" \
    "v_exp_f32 v66, v66\n\t" \
    "v_exp_f32 v67, v67\n\t" \
    "v_cvt_pk_bf16_f32 v96, v64, v65\n\t" \
    "v_cvt_pk_bf16_f32 v97, v66, v67\n\t" \
    "v_mfma_f32_32x32x8_bf16 v[80:95], v[124:125], v[26:27], v[80:95]\n\t" \
    "ds_read_b128 v[112:115], v28 offset:9216\n\t" \
    "ds_read_b64 v[124:125], v29 offset:9216\n\t" \
    "v_exp_f32 v68, v68\n\t" \
    "v_exp_f32 v69, v69\n\t" \
    "v_exp_f32 v70, v70\n\t" \
    "v_exp_f32 v71, v71\n\t" \
    "v_cvt_pk_bf16_f32 v98, v68, v69\n\t" \
    "v_cvt_pk_bf16_f32 v99, v70, v71\n\t" \
    "v_mfma_f32_32x32x16_bf16 v[48:63], v[116:119], v[104:107], v[48:63]\n\t" \
    "ds_read_b128 v[116:119], v30 offset:6144\n\t" \
    "v_exp_f32 v72, v72\n\t" \
    "v_exp_f32 v73, v73\n\t" \
    "v_exp_f32 v74, v74\n\t" \
    "v_exp_f32 v75, v75\n\t" \
    "v_cvt_pk_bf16_f32 v100, v72, v73\n\t" \
    "v_cvt_pk_bf16_f32 v101, v74, v75\n\t" \
    "v_mfma_f32_32x32x16_bf16 v[48:63], v[120:123], v[108:111], v[48:63]\n\t" \
    "ds_read_b128 v[120:123], v30 offset:6912\n\t" \
    "v_exp_f32 v76, v76\n\t" \
    "v_exp_f32 v77, v77\n\t" \
    "v_exp_f32 v78, v78\n\t" \
    "v_exp_f32 v79, v79\n\t" \
    "v_cvt_pk_bf16_f32 v102, v76, v77\n\t" \
    "v_cvt_pk_bf16_f32 v103, v78, v79\n\t" \
    "s_waitcnt lgkmcnt(3)\n\t" \
    "v_mfma_f32_32x32x16_bf16 v[64:79], v[112:115], v[16:19], 0\n\t" \
    "v_exp_f32 v80, v80\n\t" \
    "v_exp_f32 v81, v81\n\t" \
    "v_exp_f32 v82, v82\n\t" \
    "v_exp_f32 v83, v83\n\t" \
    "v_cvt_pk_bf16_f32 v104, v80, v81\n\t" \
    "v_cvt_pk_bf16_f32 v105, v82, v83\n\t" \
    "s_waitcnt lgkmcnt(2)\n\t" \
    "v_mfma_f32_32x32x8_bf16 v[64:79], v[124:125], v[24:25], v[64:79]\n\t" \
    "v_exp_f32 v84, v84\n\t" \
    "v_exp_f32 v85, v85\n\t" \
    "v_exp_f32 v86, v86\n\t" \
    "v_exp_f32 v87, v87\n\t" \
    "v_cvt_pk_bf16_f32 v106, v84, v85\n\t" \
    "v_cvt_pk_bf16_f32 v107, v86, v87\n\t" \
    "s_waitcnt lgkmcnt(1)\n\t" \
    "v_mfma_f32_32x32x16_bf16 v[32:47], v[116:119], v[96:99], v[32:47]\n\t" \
    "v_exp_f32 v88, v88\n\t" \
    "v_exp_f32 v89, v89\n\t" \
    "v_exp_f32 v90, v90\n\t" \
    "v_exp_f32 v91, v91\n\t" \
    "v_cvt_pk_bf16_f32 v108, v88, v89\n\t" \
    "v_cvt_pk_bf16_f32 v109, v90, v91\n\t" \
    "s_waitcnt lgkmcnt(0)\n\t" \
    "v_mfma_f32_32x32x16_bf16 v[32:47], v[120:123], v[100:103], v[32:47]\n\t" \
    "v_exp_f32 v92, v92\n\t" \
    "v_exp_f32 v93, v93\n\t" \
    "v_exp_f32 v94, v94\n\t" \
    "v_exp_f32 v95, v95\n\t" \
    "v_cvt_pk_bf16_f32 v110, v92, v93\n\t" \
    "v_cvt_pk_bf16_f32 v111, v94, v95\n\t" \
    "s_mov_b32 %[dl], 24576\n\t" \
    "s_cmp_eq_u32 %[ph], 2\n\t" \
    "s_cmov_b32 %[dl], -49152\n\t" \
    "s_mov_b32 m0, %[dst]\n\t" \
    "s_waitcnt vmcnt(0)\n\t" \
    "s_barrier\n\t" \
    "v_add_u32 v28, %[dl], v28\n\t" \
    "v_add_u32 v29, %[dl], v29\n\t" \
    "s_cmp_lt_u32 %[cnt], 6\n\t" \
    "s_cbranch_scc0 2f\n\t" \
    "global_load_lds_dwordx4 v31, %[src] sc1\n\t" \
    "global_load_lds_dwordx4 v31, %[src] offset:1024 sc1\n\t" \
    "global_load_lds_dwordx4 v31, %[src] offset:2048 sc1\n\t" \
    "v_add_u32 v31, 12288, v31\n\t" \
    "2:\n\t" \
    "s_mov_b32 %[t0], 24576\n\t" \
    "s_cmp_eq_u32 %[ph], 0\n\t" \
    "s_cmov_b32 %[t0], -49152\n\t" \
    "s_add_u32 %[dst], %[dst], %[t0]\n\t" \
    "s_add_u32 %[ph], %[ph], 1\n\t" \
    "s_cmp_eq_u32 %[ph], 3\n\t" \
    "s_cselect_b32 %[ph], 0, %[ph]\n\t" \
    "v_mfma_f32_32x32x16_bf16 v[80:95], v[112:115], v[20:23], 0\n\t" \
    "v_exp_f32 v64, v64\n\t" \
    "v_exp_f32 v65, v65\n\t" \
    "v_exp_f32 v66, v66\n\t" \
    "v_exp_f32 v67, v67\n\t" \
    "v_cvt_pk_bf16_f32 v96, v64, v65\n\t" \
    "v_cvt_pk_bf16_f32 v97, v66, v67\n\t" \
    "v_mfma_f32_32x32x8_bf16 v[80:95], v[124:125], v[26:27], v[80:95]\n\t" \
    "ds_read_b128 v[112:115], v28\n\t" \
    "ds_read_b64 v[124:125], v29\n\t" \
    "v_exp_f32 v68, v68\n\t" \
    "v_exp_f32 v69, v69\n\t" \
    "v_exp_f32 v70, v70\n\t" \
    "v_exp_f32 v71, v71\n\t" \
    "v_cvt_pk_bf16_f32 v98, v68, v69\n\t" \
    "v_cvt_pk_bf16_f32 v99, v70, v71\n\t" \
    "v_mfma_f32_32x32x16_bf16 v[48:63], v[116:119], v[104:107], v[48:63]\n\t" \
    "ds_read_b128 v[116:119], v30 offset:9216\n\t" \
    "v_exp_f32 v72, v72\n\t" \
    "v_exp_f32 v73, v73\n\t" \
    "v_exp_f32 v74, v74\n\t" \
    "v_exp_f32 v75, v75\n\t" \
    "v_cvt_pk_bf16_f32 v100, v72, v73\n\t" \
    "v_cvt_pk_bf16_f32 v101, v74, v75\n\t" \
    "v_mfma_f32_32x32x16_bf16 v[48:63], v[120:123], v[108:111], v[48:63]\n\t" \
    "ds_read_b128 v[120:123], v30 offset:9984\n\t" \
    "v_add_u32 v30, %[dl], v30\n\t" \
    "v_exp_f32 v76, v76\n\t" \
    "v_exp_f32 v77, v77\n\t" \
    "v_exp_f32 v78, v78\n\t" \
    "v_exp_f32 v79, v79\n\t" \
    "v_cvt_pk_bf16_f32 v102, v76, v77\n\t" \
    "v_cvt_pk_bf16_f32 v103, v78, v79\n\t" \
    "s_waitcnt lgkmcnt(3)\n\t" \
    "v_mfma_f32_32x32x16_bf16 v[64:79], v[112:115], v[16:19], 0\n\t" \
    "v_exp_f32 v80, v80\n\t" \
    "v_exp_f32 v81, v81\n\t" \
    "v_exp_f32 v82, v82\n\t" \
    "v_exp_f32 v83, v83\n\t" \
    "v_cvt_pk_bf16_f32 v104, v80, v81\n\t" \
    "v_cvt_pk_bf16_f32 v105, v82, v83\n\t" \
    "s_waitcnt lgkmcnt(2)\n\t" \
    "v_mfma_f32_32x32x8_bf16 v[64:79], v[124:125], v[24:25], v[64:79]\n\t" \
    "v_exp_f32 v84, v84\n\t" \
    "v_exp_f32 v85, v85\n\t" \
    "v_exp_f32 v86, v86\n\t" \
    "v_exp_f32 v87, v87\n\t" \
    "v_cvt_pk_bf16_f32 v106, v84, v85\n\t" \
    "v_cvt_pk_bf16_f32 v107, v86, v87\n\t" \
    "s_waitcnt lgkmcnt(1)\n\t" \
    "v_mfma_f32_32x32x16_bf16 v[32:47], v[116:119], v[96:99], v[32:47]\n\t" \
    "v_exp_f32 v88, v88\n\t" \
    "v_exp_f32 v89, v89\n\t" \
    "v_exp_f32 v90, v90\n\t" \
    "v_exp_f32 v91, v91\n\t" \
    "v_cvt_pk_bf16_f32 v108, v88, v89\n\t" \
    "v_cvt_pk_bf16_f32 v109, v90, v91\n\t" \
    "s_waitcnt lgkmcnt(0)\n\t" \
    "v_mfma_f32_32x32x16_bf16 v[32:47], v[120:123], v[100:103], v[32:47]\n\t" \
    "v_exp_f32 v92, v92\n\t" \
    "v_exp_f32 v93, v93\n\t" \
    "v_exp_f32 v94, v94\n\t" \
    "v_exp_f32 v95, v95\n\t" \
    "v_cvt_pk_bf16_f32 v110, v92, v93\n\t" \
    "v_cvt_pk_bf16_f32 v111, v94, v95\n\t" \
    "s_add_u32 %[cnt], %[cnt], 1\n\t" \
    "s_cmp_lt_u32 %[cnt], 8\n\t" \
    "s_cbranch_scc1 1b\n\t" \
    "v_mfma_f32_32x32x16_bf16 v[48:63], v[116:119], v[104:107], v[48:63]\n\t" \
    "v_mfma_f32_32x32x16_bf16 v[48:63], v[120:123], v[108:111], v[48:63]\n\t" \
    "s_nop 15\n\t"
#define ATTN_LOOP_CLOB "v64", "v65", "v66", "v67", "v68", "v69", "v70", "v71", "v72", "v73", "v74", "v75", "v76", "v77", "v78", "v79", "v80", "v81", "v82", "v83", "v84", "v85", "v86", "v87", "v88", "v89", "v90", "v91", "v92", "v93", "v94", "v95", "v96", "v97", "v98", "v99", "v100", "v101", "v102", "v103", "v104", "v105", "v106", "v107", "v108", "v109", "v110", "v111", "v112", "v113", "v114", "v115", "v116", "v117", "v118", "v119", "v120", "v121", "v122", "v123", "v124", "v125"

constexpr int AT_STAGE = 8 * KVT;
DI float max16(const f32x16& s) {
    float a = __builtin_fmaxf(s[0], s[1]);
#pragma unroll
    for (int i = 2; i < 16; ++i) a = __builtin_fmaxf(a, s[i]);
    return a;
}
DI u32x4 ld16_dev(const char* p) {
    const u64 a = ld8_dev(p), b = ld8_dev(p + 8);
    return u32x4{(unsigned)a, (unsigned)(a >> 32), (unsigned)b, (unsigned)(b >> 32)};
}
DI f32x16 attn_exact(const char* kv, bf16x8 q0, s16x4 q1, int lane) {
    const int r32 = lane & 31, h = lane >> 5;
    if (h) q1 = s16x4{0, 0, 0, 0};
    f32x16 o = {};
    float m = -INFINITY;
    const unsigned vo = 1536 + h * 384 + min(r32, 23) * 16;
#pragma unroll 1
    for (int t = 0; t < 64; ++t) {
        const char* b = kv + t * KVT;
        const bf16x8 k0 = __builtin_bit_cast(bf16x8, ld16_dev(b + lane * 16));
        const s16x4 k1 = __builtin_bit_cast(s16x4, ld8_dev(b + 1024 + lane * 8));
        const bf16x8 v0 = __builtin_bit_cast(bf16x8, ld16_dev(b + vo)), v1 = __builtin_bit_cast(bf16x8, ld16_dev(b + vo + 768));
        f32x16 s = mfma32k8(k1, q1, mfma32(k0, q0, f32x16{}));
        const float mn = __builtin_fmaxf(m, half_max(max16(s)));
        const float al = __builtin_amdgcn_exp2f(m - mn);
        m = mn;
        f32x8 lo, hi;
#pragma unroll
        for (int i = 0; i < 8; ++i) { lo[i] = __builtin_amdgcn_exp2f(s[i] - mn); hi[i] = __builtin_amdgcn_exp2f(s[8 + i] - mn); }
#pragma unroll
        for (int i = 0; i < 16; ++i) o[i] *= al;
        o = mfma32(v1, __builtin_convertvector(hi, bf16x8), mfma32(v0, __builtin_convertvector(lo, bf16x8), o));
    }
    return o;
}

DI void attn_role(const u16* __restrict__ x, const u16* __restrict__ wq, char* __restrict__ ws, unsigned lds0, int panel, int hg, bool late) {
    const int tid = threadIdx.x, lane = tid & 63, w = __builtin_amdgcn_readfirstlane(tid >> 6);
    const int r32 = lane & 31, h = lane >> 5, kh = w >> 2;
    const unsigned qt = lds0 + 2 * AT_STAGE;
    u64 tag = 0;
    TLDECL;
    __builtin_amdgcn_s_setprio(APRIO);
    if (w == 7) {
        tag = read_tag(ws);
        TL(0);
        bars(10);
        TL(1);
        poll_flags(ws + WS_FKV + r32 * FS, tag);
        TL(2);
    } else if (w >= 4) {
        const int lw = w - 4;
        const char* xb = (const char*)(x + (size_t)panel * 64 * DM);
        const char* wb = (const char*)(wq + 80 * hg);
        unsigned off[7];
#pragma unroll
        for (int j = 0; j < 7; ++j) off[j] = (lw + 3 * j < 8) ? xsrc_off(lw + 3 * j, lane, DM * 2) : w96_off(lw + 3 * j - 8, lane, 9, 63);
        unsigned d = lds0 + lw * 1024;
        if (late) __builtin_amdgcn_s_sleep(LATE);
        load_loop<7, 6>(10, lw, [&](int t) {
            const char* xs = xb + t * 128;
            const char* wsrc = wb + (size_t)t * 64 * DM * 2;
            glds16<false>(xs, off[0], d); glds16<false>(xs, off[1], d + 3072); glds16<false>(lw < 2 ? xs : wsrc, off[2], d + 6144);
#pragma unroll
            for (int j = 3; j < 6; ++j) glds16<false>(wsrc, off[j], d + j * 3072);
            if (lw < 2) glds16<false>(wsrc, off[6], d + 6 * 3072);
            d = (d >= lds0 + 2 * G96) ? d - 2 * G96 : d + G96;
        }, []() {} TLPASS(0));
    } else {
        tag = read_tag(ws);
        f32x16 a0 = {}, a1 = {};
        const unsigned qa = qt + (32 * (w & 1) + r32) * 176 + 8 * h;
        comp96(a0, a1, 9, 4, lds0, w, lane);
        if (w < 2) {
#pragma unroll
            for (int g = 0; g < 4; ++g) { lds_w64(qa + 16 * g, pack4(quad(a0, g) * QSCALE)); lds_w64(qa + 64 + 16 * g, pack4(quad(a1, g) * QSCALE)); }
        } else {
#pragma unroll
            for (int g = 0; g < 2; ++g) lds_w64(qa + 128 + 16 * g, pack4(quad(a0, g) * QSCALE));
        }
    }
    STAMP(1);
    bar_l();
    STAMP(2);
    TLDUMP(0);
    const char* kv = ws + WS_KV;
    const char* src = kv + (size_t)((w >> 2) * 32 + (w & 3)) * KVT;
#pragma unroll
    for (int t = 0; t < 2; ++t)
#pragma unroll
        for (int j = 0; j < 3; ++j) glds16<true>(src + t * 4 * KVT + j * 1024, (unsigned)lane * 16, lds0 + t * AT_STAGE + w * KVT + j * 1024);
    const unsigned qa = qt + r32 * 176 + (w & 3) * 40;
    bf16x8 qA0 = cat8(lds_b64(qa + 16 * h), lds_b64(qa + 16 * h + 8)), qB0 = cat8(lds_b64(qa + 32 * 176 + 16 * h), lds_b64(qa + 32 * 176 + 16 * h + 8));
    s16x4 qA1 = lds_b64(qa + 32), qB1 = lds_b64(qa + 32 * 176 + 32);
    if (h) { qA1 = s16x4{0, 0, 0, 0}; qB1 = s16x4{0, 0, 0, 0}; }
    waitv_bar<3>();
    const unsigned ko = lane * 16, k1o = 1024 + lane * 8, vo = 1536 + h * 384 + min(r32, 23) * 16;
    {
        const bf16x8 k0 = lds_b128(lds0 + ko);
        const s16x4 k1 = lds_b64(lds0 + k1o);
        const float mA = half_max(max16(mfma32k8(k1, qA1, mfma32(k0, qA0, f32x16{}))));
        const float mB = half_max(max16(mfma32k8(k1, qB1, mfma32(k0, qB0, f32x16{}))));
        if (h) {
            qA1[0] = (short)(__float_as_uint(-mA) >> 16);
            qB1[0] = (short)(__float_as_uint(-mB) >> 16);
        }
    }
    __builtin_amdgcn_s_setprio(0);
    STAMP(3);
    f32x16 oA, oB;
    {
        unsigned a_k = lds0 + kh * 4 * KVT + ko, a_k1 = lds0 + kh * 4 * KVT + k1o, a_v = lds0 + kh * 4 * KVT + vo;
        unsigned voff = (unsigned)lane * 16 + 2 * 4 * KVT, dst = lds0 + 2 * AT_STAGE + w * KVT, cnt, ph, dl, t0;
        asm volatile(ATTN_LOOP_ASM
                     : "={v[32:47]}"(oA), "={v[48:63]}"(oB), "+{v28}"(a_k), "+{v29}"(a_k1), "+{v30}"(a_v), "+{v31}"(voff),
                       "+{v[16:19]}"(qA0), "+{v[20:23]}"(qB0), "+{v[24:25]}"(qA1), "+{v[26:27]}"(qB1),
                       [dst] "+s"(dst), [cnt] "=&s"(cnt), [ph] "=&s"(ph), [dl] "=&s"(dl), [t0] "=&s"(t0)
                     : [src] "s"(src)
                     : ATTN_LOOP_CLOB, "scc", "memory");
    }
    STAMP(4);
    __builtin_amdgcn_s_setprio(APRIO);
    int tz = threadIdx.x;
    asm volatile("" : "+v"(tz));
    const int ln = tz & 63, r2 = tz & 31, h2 = (tz >> 5) & 1;
    const float gq = __uint_as_float(ld4_dev(ws + WS_G + (panel * 64 + 32 * kh + r2) * 4));
    bar_l();
    {
        const f32x16 snd = kh ? oA : oB;
#pragma unroll
        for (int i = 0; i < 3; ++i) *(LDS f32x4*)(lds0 + w * 3072 + ln * 16 + i * 1024) = quad(snd, i);
    }
    bar_l();
    f32x16 o = kh ? oB : oA;
#pragma unroll
    for (int i = 0; i < 3; ++i) {
        const f32x4 r = *(const LDS f32x4*)(lds0 + (w ^ 4) * 3072 + ln * 16 + i * 1024);
#pragma unroll
        for (int j = 0; j < 4; ++j) o[4 * i + j] += r[j];
    }
    {
        bool bad = h2 && !(o[8] > 1e-15f);
#pragma unroll
        for (int i = 0; i < 12; ++i) bad |= !(__builtin_fabsf(o[i]) < 1e30f);
        if (__any(bad)) o = attn_exact(kv, kh ? qB0 : qA0, kh ? qB1 : qA1, ln);
    }
    auto lr = __builtin_amdgcn_permlane32_swap(__float_as_uint(o[8]), __float_as_uint(o[8]), false, false);
    const float sc = gq * __builtin_amdgcn_rcpf(__uint_as_float(lr[1]));
    const unsigned oi = lds0 + 32768, oa = oi + (32 * kh + r2) * 176 + (w & 3) * 40 + 8 * h2;
    lds_w64(oa, pack4(quad(o, 0) * sc));
    lds_w64(oa + 16, pack4(quad(o, 1) * sc));
    if (h2 == 0) lds_w64(oa + 32, pack4(quad(o, 2) * sc));
    bar_l();
    copy_out_wt<11>(ws + WS_O + (size_t)(panel * 8 + hg) * OBLOB, oi, w, ln);
    waitv_bar<0>();
    if (tz == 0) st8_dev(ws + WS_FO + (panel * 8 + hg) * FS, tag);
    STAMP(5);
    glds16<true>(ws + WS_O + (size_t)(panel * 8 + hg) * OBLOB + w * 1024, (unsigned)ln * 16, lds0 + w * 1024);
    if (w < 3) glds16<true>(ws + WS_O + (size_t)(panel * 8 + hg) * OBLOB + (w + 8) * 1024, (unsigned)ln * 16, lds0 + (w + 8) * 1024);
}

DI void kv_role(const u16* __restrict__ x, const u16* __restrict__ wk, const u16* __restrict__ wv, const u16* __restrict__ gw,
                const u16* __restrict__ win, const u16* __restrict__ wsp, const u16* __restrict__ wo, const u16* __restrict__ wout,
                char* __restrict__ ws, unsigned lds0, int panel) {
    const int tid = threadIdx.x, lane = tid & 63, w = __builtin_amdgcn_readfirstlane(tid >> 6);
    const int l15 = lane & 15, g = lane >> 4, q = l15 >> 2, p = lane & 3;
    __builtin_amdgcn_s_setprio(3);
    constexpr int KST = 14336;
    const unsigned lg = lds0 + 5 * KST, img = lg + 2048;
    u64 tag = 0;
    TLDECL;
    f32x4 ac[2][5] = {};
    if (w == 4 || w == 5) {
        const int lw = w - 4;
        const char* xb = (const char*)(x + (size_t)panel * 64 * DM + (size_t)lw * 32 * DM);
        const char* wb = (const char*)(lw ? wv : wk);
        const unsigned xo0 = xsrc_off(0, lane, DM * 2), xo1 = xsrc_off(1, lane, DM * 2), wo2 = (unsigned)min(lane, 31) * 16 + 2048;
        unsigned d = lds0;
#pragma unroll 1
        for (int t = 0; t < 10; ++t) {
            if (t >= 5) waitv_bar<21>();
            TL(t);
            const unsigned dx = d + lw * 4096, dw = d + 8192 + lw * 3072;
            glds16<false>(xb + t * 128, xo0, dx); glds16<false>(xb + t * 128, xo1, dx + 1024);
            glds16<false>(xb + t * 128 + 16 * DM * 2, xo0, dx + 2048); glds16<false>(xb + t * 128 + 16 * DM * 2, xo1, dx + 3072);
            glds16<false>(wb + t * 2560, (unsigned)lane * 16, dw); glds16<false>(wb + t * 2560 + 1024, (unsigned)lane * 16, dw + 1024);
            glds16<false>(wb + t * 2560, wo2, dw + 2048);
            d = (d == lds0 + 4 * KST) ? lds0 : d + KST;
            if (t == 4) waitv_bar<28>();
        }
        TL(10); waitv_bar<21>(); TL(11); waitv_bar<14>(); TL(12); waitv_bar<7>(); TL(13); waitv_bar<0>(); TL(14);
    } else if (w >= 6) {
        if (w == 6) {
            glds16<false>(gw, (unsigned)lane * 16, lg);
            glds16<false>(gw, (unsigned)min(lane, 15) * 16 + 1024, lg + 1024);
        }
        tag = read_tag(ws);
        waitv_bar<0>();
        bars(9);
    } else {
        const int par = w >> 1, row = 8 * g + q;
        const unsigned xa = (unsigned)((32 * (w & 1) + l15) * 128), xs = (unsigned)(g ^ (l15 >> 1));
        const unsigned ak = 8192 + row * 40 + p * 8, am = 8192 + (p >> 1) * 3072 + row * 40 + 32;
        unsigned cur = lds0 + par * KST, ga = lg + par * 128 + g * 16;
        if (par) bar_only();
#pragma unroll 1
        for (int i = 0; i < 5; ++i) {
            bar_only();
            bf16x8 xf[2][2], w0[2], w1[2], w2[2], wg[2];
#pragma unroll
            for (int ks = 0; ks < 2; ++ks) {
                xf[ks][0] = lds_b128(cur + xa + ((xs ^ (4 * ks)) << 4));
                xf[ks][1] = lds_b128(cur + xa + 2048 + ((xs ^ (4 * ks)) << 4));
                w0[ks] = cat8(lds_tr(cur + ak + ks * 1280), lds_tr(cur + ak + ks * 1280 + 160));
                w2[ks] = cat8(lds_tr(cur + ak + 3072 + ks * 1280), lds_tr(cur + ak + 3072 + ks * 1280 + 160));
                w1[ks] = cat8(lds_tr(cur + am + ks * 1280), lds_tr(cur + am + ks * 1280 + 160));
                wg[ks] = lds_b128(ga + ks * 64);
            }
            SB();
            asm volatile("s_waitcnt lgkmcnt(0)" ::: "memory");
            if (i < 4 || !par) bar_only();
            SB();
#pragma unroll
            for (int ks = 0; ks < 2; ++ks)
#pragma unroll
                for (int m = 0; m < 2; ++m) {
                    ac[m][0] = mfma16(w0[ks], xf[ks][m], ac[m][0]);
                    ac[m][1] = mfma16(w1[ks], xf[ks][m], ac[m][1]);
                    ac[m][2] = mfma16(xf[ks][m], w2[ks], ac[m][2]);
                    ac[m][3] = mfma16(xf[ks][m], w1[ks], ac[m][3]);
                    ac[m][4] = mfma16(wg[ks], xf[ks][m], ac[m][4]);
                }
            cur += 2 * KST;
            if (cur >= lds0 + 5 * KST) cur -= 5 * KST;
            ga += 256;
        }
        const unsigned ra = lds0 + w * 5120 + lane * 16;
#pragma unroll
        for (int j = 0; j < 5; ++j) *(LDS f32x4*)(ra + j * 1024) = par ? ac[0][j] : ac[1][j];
    }
    bar_l();
    TL(15);
    if (w < 4) {
        const int par = w >> 1;
        const unsigned ra = lds0 + (w ^ 2) * 5120 + lane * 16;
        f32x4 a[5];
#pragma unroll
        for (int j = 0; j < 5; ++j) a[j] = (par ? ac[1][j] : ac[0][j]) + *(const LDS f32x4*)(ra + j * 1024);
        const unsigned kb = img + (w & 1) * KVT;
        const int key = 16 * par + l15;
        lds_w64(kb + (key + 32 * (g >> 1)) * 16 + (g & 1) * 8, pack4(a[0]));
        if (g < 2) lds_w64(kb + 1024 + g * 256 + key * 8, g ? u32x2{0x00003f80u, 0u} : pack4(a[1]));
        if (g == 0)
            __hip_atomic_store((GAS unsigned*)(ws + WS_G + (panel * 64 + 32 * (w & 1) + key) * 4),
                               __float_as_uint(__builtin_amdgcn_rcpf(1.0f + __builtin_amdgcn_exp2f(-1.4426950408889634f * a[4][0]))), RLX_AGENT);
        const unsigned vb = kb + 1536 + par * 768 + (g & 1) * 384 + (g >> 1) * 8;
        lds_w64(vb + l15 * 16, pack4(a[2]));
        if (l15 >= 8) {
            u32x2 val = pack4(a[3]);
            if (l15 == 12) val = u32x2{0x3f803f80u, 0x3f803f80u};
            if (l15 > 12) val = u32x2{0u, 0u};
            lds_w64(vb + (8 + l15) * 16, val);
        }
    }
    bar_l();
    TL(16);
    copy_out_wt<6>(ws + WS_KV + (size_t)panel * 2 * KVT, img, w, lane);
    waitv_bar<0>();
    TL(17);
    TLDUMP(0);
    if (tid == 448) st8_dev(ws + WS_FKV + panel * FS, tag);
    STAMP(5);
    __builtin_amdgcn_s_setprio(0);
    if (w < 6) tag = read_tag(ws);
    {
        const int eng = (panel & 3) * 8 + w;
        const unsigned sink = lds0 + w * 1024;
        {
            const int c = eng * 64 + lane;
#pragma unroll 1
            for (int i = 0; i < 34; ++i) {
                const int cc = min(c + i * 2048, 640 * 108 - 1), row = cc / 108;
                glds16<false>(win, (unsigned)(row * F2 * 2 + (cc - row * 108) * 16), sink);
            }
        }
        pull_l2<false>((const char*)(wsp + (size_t)panel * 64 * SEQ), w, 8, 256, sink, lane);
#pragma unroll 1
        for (int i = 0; i < 4; ++i) poll_flags(ws + WS_FZN + min(64 * i + lane, 223) * FS, tag);
        pull_l2<true>(ws + WS_ZN, eng, 32, 32 * 7 * 16, sink, lane);
        pull_l2<false>((const char*)wout, eng, 32, FH * DM * 2 / 1024, sink, lane);
        pull_l2<false>((const char*)wo, eng, 32, DM * DM * 2 / 1024, sink, lane);
    }
    if (panel == 0 && w == 0) {
#pragma unroll 1
        for (int i = 0; i < 4; ++i) poll_flags(ws + WS_FZN + min(64 * i + lane, 223) * FS, tag);
        poll_flags(ws + WS_FKV + (lane & 31) * FS, tag);
#pragma unroll 1
        for (int i = 0; i < 4; ++i) poll_flags(ws + WS_FO + (64 * i + lane) * FS, tag);
        if (lane == 0) st8_dev(ws + WS_E, tag);
    }
}

DI void gemm_role(const u16* __restrict__ x, const u16* __restrict__ win, const u16* __restrict__ wsp, const u16* __restrict__ bsp,
                  const u16* __restrict__ wo, const u16* __restrict__ wout, char* __restrict__ ws, u16* __restrict__ out,
                  unsigned lds0, int panel, int nt) {
    const int tid = threadIdx.x, lane = tid & 63, w = __builtin_amdgcn_readfirstlane(tid >> 6);
    const unsigned stg = lds0 + 2 * G128;
    const int me = panel * 7 + nt, maxc = nt == 6 ? 7 : 11;
    u64 tag = 0;
    TLDECL;
    __builtin_amdgcn_s_setprio(CPRIO);
    if (w == 7) {
        tag = read_tag(ws);
        bars(11);
        TL(0);
        poll_flags(ws + WS_FKV + panel * FS, tag);
        TL(1);
        bars(2);
        TL(2);
        {
            const char* sp = ws + WS_ST + (size_t)panel * 7 * 2048;
            const unsigned gv = ld4_dev(ws + WS_G + (panel * 64 + lane) * 4);
            const u16 bv = bsp[panel * 64 + lane];
            float s1, s2;
#pragma unroll 1
            for (unsigned spins = 0; spins < (1u << 18); ++spins) {
                u32x4 pr[14];
#pragma unroll
                for (int i = 0; i < 14; ++i) pr[i] = ld16_issue(sp + i * 1024, (unsigned)lane * 16);
                if (spins == 0) bar_only();
                WAIT14(pr);
                bool ok = true;
                s1 = 0.f; s2 = 0.f;
#pragma unroll
                for (int i = 0; i < 14; ++i) {
                    ok &= pr[i][1] == (unsigned)tag && pr[i][3] == (unsigned)tag;
                    s1 += __uint_as_float(pr[i][0]); s2 += __uint_as_float(pr[i][2]);
                }
                if (__all(ok)) break;
            }
            TL(3);
            const float mean = s1 * (1.0f / FH);
            const float rstd = __builtin_amdgcn_rsqf(__builtin_fmaxf(s2 * (1.0f / FH) - mean * mean, 0.f) + 1e-5f);
            *(LDS f32x4*)(lds0 + SPARE + lane * 16) = f32x4{mean, rstd, 1.0f - __uint_as_float(gv), bf2f(bv)};
            bar_l();
        }
        bars(4);
        if (lane == 0) st8_dev(ws + WS_FZN + (nt * 32 + panel) * FS, tag);
        TL(4);
        bars(2 + 32);
        bar_l();
        copy_out_wt<16>(ws + WS_U + (size_t)me * 16384, stg, w, lane);
        waitv_bar<0>();
        if (lane == 0) st8_dev(ws + WS_FU + (panel * 8 + nt) * FS, tag);
        pull_l2<true>(ws + WS_U + (size_t)me * 16384, 0, 1, 16, lds0 + SPARE + 1024, lane);
        bars(14);
        poll_flags(ws + WS_FO + (panel * 8 + (lane & 7)) * FS, tag);
        waitv_bar<0>();
        bars(8);
        TLDUMP(0);
    } else if (w >= 4) {
        const int lw = w - 4;
        unsigned d = lds0 + lw * 1024;
        auto ring3 = [&](int sz) { d = (d >= lds0 + 2 * sz) ? d - 2 * sz : d + sz; };
        {
            const char* xb = (const char*)(x + (size_t)panel * 64 * DM);
            const char* wb = (const char*)(win + nt * 128);
            unsigned off[8];
#pragma unroll
            for (int j = 0; j < 8; ++j)
                off[j] = (lw + 3 * j < 8) ? xsrc_off(lw + 3 * j, lane, DM * 2)
                                          : (unsigned)((4 * (lw + 3 * j - 8) + (lane >> 4)) * F2 * 2 + (min((lane & 15) ^ ((lane >> 4) << 2), nt == 6 ? 11 : 15) << 4));
            load_loop<8, 8>(20, lw, [&](int t) {
                const int k = t < 10 ? t : t - 10;
                const char* xs = xb + k * 128;
                const char* wsrc = wb + (size_t)k * 64 * F2 * 2 + (t < 10 ? FH * 2 : 0);
                glds16<false>(xs, off[0], d); glds16<false>(xs, off[1], d + 3072); glds16<false>(lw < 2 ? xs : wsrc, off[2], d + 6144);
#pragma unroll
                for (int j = 3; j < 8; ++j) glds16<false>(wsrc, off[j], d + j * 3072);
                ring3(G128);
            }, [&]() { tag = read_tag(ws); } TLPASS(0));
        }
        bar_only();
        poll_flags(ws + WS_FZN + (nt * 32 + (lane & 31)) * FS, tag);
        {
            const char* xb = (const char*)(wsp + (size_t)panel * 64 * SEQ);
            const char* wb = ws + WS_ZN + (size_t)nt * 16384 + (lw - 8) * 1024;
            const unsigned xo0 = xsrc_off(lw, lane, SEQ * 2), xo1 = xsrc_off(lw + 3, lane, SEQ * 2);
            const unsigned xo2 = lw < 2 ? xsrc_off(lw + 6, lane, SEQ * 2) : (unsigned)lane * 16;
            d = lds0 + lw * 1024;
            load_loop<8, 8>(32, lw, [&](int t) {
                const char* xs = xb + t * 128;
                const char* wsrc = wb + (size_t)t * 7 * 16384;
                glds16<false>(xs, xo0, d); glds16<false>(xs, xo1, d + 3072); glds16<true>(lw < 2 ? xs : wsrc + 6144, xo2, d + 6144);
#pragma unroll
                for (int j = 3; j < 8; ++j) glds16<true>(wsrc + j * 3072, (unsigned)lane * 16, d + j * 3072);
                ring3(G128);
            }, []() {} TLPASS(22));
        }
        TLDUMP(0);
        bar_l();
        copy_out_wt<16>(ws + WS_U + (size_t)me * 16384, stg, w, lane);
        waitv_bar<0>();
        TL(40);
        poll_flags(ws + WS_FU + (panel * 8 + min(lane, 6)) * FS, tag);
        {
            const char* xb = ws + WS_U + (size_t)panel * 7 * 16384 + lw * 1024;
            const char* wb = (const char*)(wout + nt * 96);
            unsigned wf[5], wl[5];
#pragma unroll
            for (int j = 0; j < 5; ++j) { wf[j] = w96_off(max(lw + 3 * j - 2, 0), lane, maxc, 63); wl[j] = w96_off(max(lw + 3 * j - 2, 0), lane, maxc, 31); }
            if (lw < 2) wf[0] = wl[0] = (unsigned)lane * 16;
            d = lds0 + lw * 1024;
            load_loop<7, 6>(14, lw, [&](int t) {
                const char* xs = xb + t * 8192;
                const char* wsrc = wb + (size_t)t * 64 * DM * 2;
                if (t == 13) {
#pragma unroll
                    for (int j = 0; j < 5; ++j) wf[j] = wl[j];
                }
                glds16<true>(xs, (unsigned)lane * 16, d); glds16<true>(xs + 3072, (unsigned)lane * 16, d + 3072);
                glds16<true>(lw < 2 ? xs + 6144 : wsrc, wf[0], d + 6144);
#pragma unroll
                for (int j = 1; j < 4; ++j) glds16<false>(wsrc, wf[j], d + 6144 + j * 3072);
                if (lw < 2) glds16<false>(wsrc, wf[4], d + 6144 + 4 * 3072);
                ring3(G96);
            }, []() {} TLPASS(0));
        }
        bar_l();
        {
            const char* xb = ws + WS_O + (size_t)panel * 8 * OBLOB + lw * 1024;
            const char* wb = (const char*)(wo + nt * 96);
            unsigned wf[6];
#pragma unroll
            for (int j = 0; j < 6; ++j) wf[j] = w96_off(min(max(lw + 3 * j - 2, 0), 14), lane, maxc, 79);
            if (lw < 2) wf[0] = (unsigned)lane * 16;
            d = lds0 + lw * 1024;
            load_loop<9, 8>(8, lw, [&](int t) {
                const char* xs = xb + t * OBLOB;
                const char* wsrc = wb + (size_t)t * 80 * DM * 2;
#pragma unroll
                for (int j = 0; j < 3; ++j) glds16<true>(xs + j * 3072, (unsigned)lane * 16, d + j * 3072);
                glds16<true>(lw < 2 ? xs + 9216 : wsrc, wf[0], d + 9216);
#pragma unroll
                for (int j = 1; j < 5; ++j) glds16<false>(wsrc, wf[j], d + 9216 + j * 3072);
                if (lw < 2) glds16<false>(wsrc, wf[5], d + 9216 + 5 * 3072);
                ring3(G80);
            }, []() {} TLPASS(16));
            TLDUMP(1);
        }
    } else {
        tag = read_tag(ws);
        const int tm = w & 1, tn2 = w >> 1;
        const bool two = !(nt == 6 && tn2 == 1);
        u32x2 zp[8];
        f32x16 a0 = {}, a1 = {};
        int n = 9;
#define RUN128(ASM, ln) do { const Addr128 ad = addr128(lds0, w, ln); \
            asm volatile(ASM : "+{v[32:47]}"(a0), "+{v[48:63]}"(a1), [n] "+s"(n) : ADDR128_OPS(ad) : CLOB_F48, "scc", "memory"); } while (0)
        RUN128(ASM_E1, lane);
        STAMP(1);
        {
            const int ln = olane(), tokl = 32 * tm + (ln & 31);
            float s1 = 0.f, s2 = 0.f;
#pragma unroll
            for (int i = 0; i < 8; ++i) {
                zp[i] = gelu_pack(quad(i < 4 ? a0 : a1, i & 3));
                if (i >= 4 && !two) zp[i] = u32x2{0u, 0u};
                const f32x4 v = unpack4(zp[i]);
                s1 += (v[0] + v[1]) + (v[2] + v[3]);
                s2 += (v[0] * v[0] + v[1] * v[1]) + (v[2] * v[2] + v[3] * v[3]);
                asm volatile("" : "+v"(zp[i]));
            }
            s1 = half_sum(s1); s2 = half_sum(s2);
            st8_dev(ws + WS_ST + (size_t)(me * 2 + tn2) * 1024 + tokl * 16 + (ln >> 5) * 8, (tag << 32) | __float_as_uint(ln < 32 ? s1 : s2));
            a0 = f32x16{}; a1 = f32x16{};
            n = 4;
            RUN128(ASM_E2, ln);
        }
        STAMP(2);
        {
            const int ln = olane(), h = ln >> 5, tokl = 32 * tm + (ln & 31);
            const f32x4 pr = *(const LDS f32x4*)(lds0 + SPARE + tokl * 16);
#pragma unroll
            for (int i = 0; i < 8; i += 2) {
                u32x2 v0 = pack4((unpack4(zp[i]) - pr[0]) * pr[1]), v1 = pack4((unpack4(zp[i + 1]) - pr[0]) * pr[1]);
                if (i >= 4 && !two) { v0 = u32x2{0u, 0u}; v1 = v0; }
                const auto sx = __builtin_amdgcn_permlane32_swap(v0[0], v1[0], false, false), sy = __builtin_amdgcn_permlane32_swap(v0[1], v1[1], false, false);
                st16_wt(ws + WS_ZN + (size_t)me * 16384, (unsigned)(tokl * 256 + (((8 * tn2 + i + h) ^ ((tokl & 3) << 2)) << 4)), u32x4{sx[0], sy[0], sx[1], sy[1]});
            }
            STAMP(3);
            n = 4;
            RUN128(ASM_E1, ln);
        }
        bar_only();
        {
#pragma unroll
            for (int i = 0; i < 8; ++i) { zp[i] = gelu_pack(quad(i < 4 ? a0 : a1, i & 3)); asm volatile("" : "+v"(zp[i])); }
            a0 = f32x16{}; a1 = f32x16{};
            n = 31;
            STAMP(4);
            RUN128(ASM_E1, olane());
        }
        STAMP(5);
        {
            const int ln = olane(), h = ln >> 5, tokl = 32 * tm + (ln & 31);
            const f32x4 pr = *(const LDS f32x4*)(lds0 + SPARE + tokl * 16);
#pragma unroll
            for (int i = 0; i < 8; ++i)
                lds_w64(stg + tn2 * 8192 + tokl * 128 + ((i ^ ((tokl >> 1) & 7)) << 4) + 8 * h, pack4((quad(i < 4 ? a0 : a1, i & 3) + pr[3]) * unpack4(zp[i]) * pr[2]));
            bar_l();
            copy_out_wt<16>(ws + WS_U + (size_t)me * 16384, stg, w, ln);
            waitv_bar<0>();
            STAMP(6);
            a0 = f32x16{}; a1 = f32x16{};
            comp96(a0, a1, 13, 2, lds0, w, ln);
        }
        bar_l();
        STAMP(7);
        comp80(a0, a1, 7, lds0, w, olane());
        const int ln = olane();
        const unsigned oa = lds0 + (32 * tm + (ln & 31)) * 208 + 8 * (ln >> 5);
        if (w < 2) {
#pragma unroll
            for (int g = 0; g < 4; ++g) { lds_w64(oa + 16 * g, pack4(quad(a0, g))); lds_w64(oa + 64 + 16 * g, pack4(quad(a1, g))); }
        } else {
#pragma unroll
            for (int g = 0; g < 4; ++g) lds_w64(oa + 128 + 16 * g, pack4(quad(a0, g)));
        }
    }
    bar_l();
#pragma unroll
    for (int j = 0; j < 2; ++j) {
        const int c = (w + 8 * j) * 64 + olane(), row = c / 12, pos = c - row * 12;
        if (w + 8 * j < 12 && nt * 96 + pos * 8 < DM)
            *(u32x4*)(out + (size_t)(panel * 64 + row) * DM + nt * 96 + pos * 8) = lds_u128(lds0 + row * 208 + pos * 16);
    }
    STAMP(8);
}

__global__ __launch_bounds__(512, 4) void fused_kernel(const u16* __restrict__ x, const u16* __restrict__ wq, const u16* __restrict__ wk,
                                                        const u16* __restrict__ wv, const u16* __restrict__ wo, const u16* __restrict__ win,
                                                        const u16* __restrict__ wsp, const u16* __restrict__ bsp, const u16* __restrict__ wout,
                                                        const u16* __restrict__ gw, char* __restrict__ ws, u16* __restrict__ out) {
    extern __shared__ __attribute__((aligned(16))) char smem[];
    const unsigned lds0 = (unsigned)(uintptr_t)smem;
    STAMP(0);
    asm volatile("" ::"s"(x), "s"(wq), "s"(wk), "s"(wv), "s"(wo), "s"(win), "s"(wsp), "s"(bsp), "s"(wout), "s"(gw), "s"(ws), "s"(out));
    const int bid = blockIdx.x, xg = bid & 7;
    if (bid < 256) {
        attn_role(x, wq, ws, lds0, xg * 4 + (bid >> 6), (bid >> 3) & 7, bid >= 224);
    } else {
        const int j = (bid - 256) >> 3;
        if (j < 28) gemm_role(x, win, wsp, bsp, wo, wout, ws, out, lds0, xg * 4 + j / 7, j % 7);
        else kv_role(x, wk, wv, gw, win, wsp, wo, wout, ws, lds0, xg * 4 + (j - 28));
    }
}

extern "C" void kernel_launch(void* const* d_in, const int* in_sizes, int n_in, void* d_out, int out_size, void* d_ws, size_t ws_size,
                              hipStream_t stream) {
    (void)hipFuncSetAttribute((const void*)fused_kernel, hipFuncAttributeMaxDynamicSharedMemorySize, LDS_BYTES);
    fused_kernel<<<512, 512, LDS_BYTES, stream>>>((const u16*)d_in[0], (const u16*)d_in[1], (const u16*)d_in[2], (const u16*)d_in[3],
                                                   (const u16*)d_in[4], (const u16*)d_in[6], (const u16*)d_in[7], (const u16*)d_in[8],
                                                   (const u16*)d_in[9], (const u16*)d_in[10], (char*)d_ws, (u16*)d_out);
}
```
